```python
import torch

def _softmax(x, axis=-1):
    return torch.softmax(x, dim=axis)
def _gelu(x):
    return torch.nn.functional.gelu(x, approximate="tanh")
def _silu(x):
    return torch.nn.functional.silu(x)
def _sigmoid(x):
    return torch.sigmoid(x)
def _layernorm(x, axis=-1, eps=1e-5):
    m = x.mean(dim=axis, keepdim=True)
    v = x.var(dim=axis, keepdim=True, unbiased=False)
    return (x - m) / torch.sqrt(v + eps)
def _scatter_add(tgt, idx, src, axis=0):
    return tgt.clone().index_add_(axis, idx, src)
def _segment_sum(src, seg, n):
    out = torch.zeros((n,) + src.shape[1:], dtype=src.dtype)
    return out.index_add_(0, seg, src)
def _pad(x, pw):
    flat = tuple(v for pair in reversed(pw) for v in pair)
    return torch.nn.functional.pad(x, flat)

kernel_name = 'gated_mix__linear_kernel_hedgehog__ssm_conv_s4__prompt'

def setup_inputs():

    _w = (lambda a: (lambda t: t.to(torch.bfloat16) if t.is_floating_point() else t)(torch.as_tensor(a)))
    def _A_setup():
        B, S, D, H = 1, 2048, 448, 16
        Dh = D // H
        return {
            "x": _w((lambda *s: torch.randn(*s, dtype=torch.bfloat16) * 0.05)(B, S, D)),
            "wqkv": _w((lambda *s: torch.randn(*s, dtype=torch.bfloat16) * 0.8)(D, 3 * D)),
            "w_phi": _w((lambda *s: torch.randn(*s, dtype=torch.bfloat16) * 0.8)(H, Dh, Dh) * (Dh ** -0.5)),
            "wo": _w((lambda *s: torch.randn(*s, dtype=torch.bfloat16) * 0.05)(D, D)),
        }
    _A_in = _A_setup()

    def _B_setup():
        B, L, D, N = 1, 2048, 448, 256
        A_re = -(lambda _x: torch.exp(torch.as_tensor(_x)))((lambda *s: torch.randn(*s, dtype=torch.bfloat16) * 6.4)(D, N))
        A_im = torch.pi * (lambda *s: torch.randn(*s, dtype=torch.bfloat16) * 0.05)(D, N)
        B_re = (lambda *s: torch.randn(*s, dtype=torch.bfloat16) * 0.1)(D, N)
        B_im = (lambda *s: torch.randn(*s, dtype=torch.bfloat16) * 0.1)(D, N)
        C_re = (lambda *s: torch.randn(*s, dtype=torch.bfloat16) * 0.1)(D, N)
        C_im = (lambda *s: torch.randn(*s, dtype=torch.bfloat16) * 0.1)(D, N)
        log_dt = (lambda *s: torch.randn(*s, dtype=torch.bfloat16) * 0.05)(D) * 0.5 - 4.0
        Dskip = (lambda *s: torch.randn(*s, dtype=torch.bfloat16) * 0.05)(D)
        return {
            "u": _w((lambda *s: torch.randn(*s, dtype=torch.bfloat16) * 0.05)(B, L, D)),
            "A_re": _w(A_re),
            "A_im": _w(A_im),
            "B_re": _w(B_re),
            "B_im": _w(B_im),
            "C_re": _w(C_re),
            "C_im": _w(C_im),
            "log_dt": _w(log_dt),
            "Dskip": _w(Dskip),
        }
    _B_in = _B_setup()

    return {
        "A_x": _A_in["x"],
        "A_wqkv": _A_in["wqkv"],
        "A_w_phi": _A_in["w_phi"],
        "A_wo": _A_in["wo"],
        "B_u": _B_in["u"],
        "B_A_re": _B_in["A_re"],
        "B_A_im": _B_in["A_im"],
        "B_B_re": _B_in["B_re"],
        "B_B_im": _B_in["B_im"],
        "B_C_re": _B_in["C_re"],
        "B_C_im": _B_in["C_im"],
        "B_log_dt": _B_in["log_dt"],
        "B_Dskip": _B_in["Dskip"],
        "gate_w": _w((lambda *s: torch.randn(*s, dtype=torch.bfloat16) * 0.05)(448) * 16.0),
    }

def reference(A_x, A_wqkv, A_w_phi, A_wo, B_u, B_A_re, B_A_im, B_B_re, B_B_im, B_C_re, B_C_im, B_log_dt, B_Dskip, gate_w):
    def _A_ref(x, wqkv, w_phi, wo):
        B, S, D = x.shape
        H = 16; Dh = D // H
        qkv = (lambda x, *d: x.permute(*d))((x @ wqkv).reshape(B, S, 3, H, Dh), 2, 0, 3, 1, 4)
        q, k, v = qkv[0], qkv[1], qkv[2]
        pq = torch.einsum("bhsd,hdf->bhsf", q, w_phi)
        pk = torch.einsum("bhsd,hdf->bhsf", k, w_phi)
        phi_q = torch.concatenate([_softmax(pq, axis=-1), _softmax(-pq, axis=-1)], axis=-1)
        phi_k = torch.concatenate([_softmax(pk, axis=-1), _softmax(-pk, axis=-1)], axis=-1)
        kv = torch.einsum("bhsf,bhsd->bhfd", phi_k, v)
        z  = torch.einsum("bhsf->bhf", phi_k)
        num = torch.einsum("bhqf,bhfd->bhqd", phi_q, kv)
        den = torch.einsum("bhqf,bhf->bhq", phi_q, z)[..., None] + 1e-6
        o = num / den
        return (lambda x, *d: x.permute(*d))(o, 0, 2, 1, 3).reshape(B, S, D) @ wo

    def _B_ref(u, A_re, A_im, B_re, B_im, C_re, C_im, log_dt, Dskip):
        Bsz, L, D = u.shape
        dt = torch.exp(log_dt)[:, None]
        A = A_re + 1j * A_im
        Bc = B_re + 1j * B_im
        Cc = C_re + 1j * C_im
        logdA = dt * A
        dB = dt * Bc
        l = (lambda x, dt: x.to(dt))(torch.arange(L), A_re.dtype)
        P = torch.exp(logdA[:, :, None] * l[None, None, :])
        K = torch.einsum("dn,dnl,dn->dl", Cc, P, dB).real
        L2 = 2 * L
        Kp = _pad(K, ((0, 0), (0, L)))
        up = (lambda x, *d: x.permute(*d))(u, 0, 2, 1)
        up = _pad(up, ((0, 0), (0, 0), (0, L)))
        Kf = (lambda x, axes=(-2,-1): torch.fft.rfft2(x.float(), dim=axes))(Kp[:, None, :], axes=(-2, -1))
        Uf = (lambda x, axes=(-2,-1): torch.fft.rfft2(x.float(), dim=axes))(up[:, :, None, :], axes=(-2, -1))
        Yf = Uf * Kf[None, :, :, :]
        y = (lambda x, s, axes=(-2,-1): torch.fft.irfft2(x, s=s, dim=axes).to(torch.bfloat16))(Yf, (1, L2), axes=(-2, -1))[:, :, 0, :L]
        y = (lambda x, *d: x.permute(*d))(y, 0, 2, 1) + u * Dskip[None, None, :]
        return y

    x = A_x
    g = _sigmoid((x * gate_w).sum(-1, keepdims=True))
    a = _A_ref(x, A_wqkv, A_w_phi, A_wo)
    b = _B_ref(x, B_A_re, B_A_im, B_B_re, B_B_im, B_C_re, B_C_im, B_log_dt, B_Dskip)
    return g * a + (1 - g) * b
```

```cpp
#include <hip/hip_runtime.h>
#include <hip/hip_bf16.h>
#include <stdint.h>

typedef unsigned short u16;
constexpr int S = 2048, D = 448, H = 16, DH = 28, NS = 256, L = 2048;

__device__ __forceinline__ float bf2f(u16 v) { return __uint_as_float(((unsigned)v) << 16); }
__device__ __forceinline__ u16 f2bf(float f) { unsigned u = __float_as_uint(f); u += 0x7fffu + ((u >> 16) & 1u); return (u16)(u >> 16); }
__device__ __forceinline__ float rbf(float f) { return bf2f(f2bf(f)); }

__global__ void k_gate(const u16* x, const u16* gw, float* g) {
  int l = blockIdx.x; int t = threadIdx.x;
  float s = 0.f;
  for (int d = t; d < D; d += 64) s += rbf(bf2f(x[l * D + d]) * bf2f(gw[d]));
  for (int m = 32; m >= 1; m >>= 1) s += __shfl_xor(s, m);
  if (t == 0) { float sb = rbf(s); g[l] = rbf(1.f / (1.f + expf(-sb))); }
}
__global__ void k_gemm(const u16* A, const u16* B, u16* C, int M, int N, int K) {
  int n = blockIdx.x * 64 + threadIdx.x; int m = blockIdx.y;
  if (n >= N) return;
  float s = 0.f;
  for (int k = 0; k < K; ++k) s += bf2f(A[m * K + k]) * bf2f(B[k * N + n]);
  C[m * N + n] = f2bf(s);
}
__global__ void k_phi(const u16* qkv, const u16* wphi, u16* phi) {
  int idx = blockIdx.x * blockDim.x + threadIdx.x;
  if (idx >= 2 * S * H) return;
  int which = idx / (S * H); int r = idx % (S * H); int l = r / H; int h = r % H;
  float p[DH];
  float mx = -1e30f, mn = 1e30f;
  for (int f = 0; f < DH; ++f) {
    float s = 0.f;
    for (int d = 0; d < DH; ++d) s += bf2f(qkv[l * 3 * D + which * D + h * DH + d]) * bf2f(wphi[(h * DH + d) * DH + f]);
    s = rbf(s); p[f] = s; mx = fmaxf(mx, s); mn = fminf(mn, s);
  }
  float sp = 0.f, sn = 0.f;
  for (int f = 0; f < DH; ++f) { sp += expf(p[f] - mx); sn += expf(mn - p[f]); }
  u16* o = phi + ((size_t)(which * S + l) * H + h) * 56;
  for (int f = 0; f < DH; ++f) { o[f] = f2bf(expf(p[f] - mx) / sp); o[DH + f] = f2bf(expf(mn - p[f]) / sn); }
}
__global__ void k_kv(const u16* qkv, const u16* phi, float* kv) {
  int idx = blockIdx.x * blockDim.x + threadIdx.x;
  if (idx >= H * 56 * 29) return;
  int h = idx / (56 * 29); int r = idx % (56 * 29); int f = r / 29; int d = r % 29;
  float s = 0.f;
  for (int l = 0; l < S; ++l) {
    float pk = bf2f(phi[((size_t)(S + l) * H + h) * 56 + f]);
    float v = d < 28 ? bf2f(qkv[l * 3 * D + 2 * D + h * DH + d]) : 1.f;
    s += pk * v;
  }
  kv[idx] = rbf(s);
}
__global__ void k_o(const u16* phi, const float* kv, u16* o) {
  int idx = blockIdx.x * blockDim.x + threadIdx.x;
  if (idx >= S * H * DH) return;
  int l = idx / D; int c = idx % D; int h = c / DH; int d = c % DH;
  float num = 0.f, den = 0.f;
  for (int f = 0; f < 56; ++f) {
    float pq = bf2f(phi[((size_t)l * H + h) * 56 + f]);
    num += pq * kv[(h * 56 + f) * 29 + d];
    den += pq * kv[(h * 56 + f) * 29 + 28];
  }
  num = rbf(num); den = rbf(rbf(den) + 1e-6f);
  o[idx] = f2bf(num / den);
}
__global__ void k_K(const u16* Are, const u16* Aim, const u16* Bre, const u16* Bim, const u16* Cre, const u16* Cim, const u16* logdt, float* K) {
  int d = blockIdx.y; int l = blockIdx.x * 64 + threadIdx.x;
  float dt = rbf(expf(bf2f(logdt[d])));
  float lb = rbf((float)l);
  float s = 0.f;
  for (int n = 0; n < NS; ++n) {
    float ar = dt * bf2f(Are[d * NS + n]), ai = dt * bf2f(Aim[d * NS + n]);
    float br = dt * bf2f(Bre[d * NS + n]), bi = dt * bf2f(Bim[d * NS + n]);
    float cr = bf2f(Cre[d * NS + n]), ci = bf2f(Cim[d * NS + n]);
    float e = expf(ar * lb); float sn, cs; sincosf(ai * lb, &sn, &cs);
    float pr = e * cs, pi = e * sn;
    float tr = cr * pr - ci * pi, ti = cr * pi + ci * pr;
    s += tr * br - ti * bi;
  }
  K[d * L + l] = s;
}
__global__ void k_conv(const u16* x, const float* K, const u16* Dsk, u16* b) {
  int d = blockIdx.y; int l = blockIdx.x * 64 + threadIdx.x;
  float s = 0.f;
  for (int j = 0; j <= l; ++j) s += K[d * L + j] * bf2f(x[(l - j) * D + d]);
  float y = rbf(s);
  float sk = rbf(bf2f(x[l * D + d]) * bf2f(Dsk[d]));
  b[l * D + d] = f2bf(y + sk);
}
__global__ void k_final(const u16* a, const u16* b, const float* g, u16* out) {
  int idx = blockIdx.x * blockDim.x + threadIdx.x;
  if (idx >= S * D) return;
  int l = idx / D;
  float gg = g[l];
  float t1 = rbf(gg * bf2f(a[idx]));
  float t2 = rbf(rbf(1.f - gg) * bf2f(b[idx]));
  out[idx] = f2bf(t1 + t2);
}

extern "C" void kernel_launch(void* const* d_in, const int* in_sizes, int n_in, void* d_out, int out_size, void* d_ws, size_t ws_size, hipStream_t stream) {
  const u16* x = (const u16*)d_in[0]; const u16* wqkv = (const u16*)d_in[1]; const u16* wphi = (const u16*)d_in[2]; const u16* wo = (const u16*)d_in[3];
  const u16* Are = (const u16*)d_in[5]; const u16* Aim = (const u16*)d_in[6]; const u16* Bre = (const u16*)d_in[7]; const u16* Bim = (const u16*)d_in[8];
  const u16* Cre = (const u16*)d_in[9]; const u16* Cim = (const u16*)d_in[10]; const u16* logdt = (const u16*)d_in[11]; const u16* Dsk = (const u16*)d_in[12]; const u16* gw = (const u16*)d_in[13];
  char* ws = (char*)d_ws;
  float* g = (float*)ws; ws += 1 << 20;
  u16* qkv = (u16*)ws; ws += 8 << 20;
  u16* phi = (u16*)ws; ws += 16 << 20;
  float* kv = (float*)ws; ws += 1 << 20;
  u16* o = (u16*)ws; ws += 4 << 20;
  float* K = (float*)ws; ws += 8 << 20;
  u16* b = (u16*)ws; ws += 4 << 20;
  u16* a = (u16*)ws; ws += 4 << 20;
  k_gate<<<S, 64, 0, stream>>>(x, gw, g);
  k_gemm<<<dim3(3 * D / 64, S), 64, 0, stream>>>(x, wqkv, qkv, S, 3 * D, D);
  k_phi<<<(2 * S * H + 255) / 256, 256, 0, stream>>>(qkv, wphi, phi);
  k_kv<<<(H * 56 * 29 + 63) / 64, 64, 0, stream>>>(qkv, phi, kv);
  k_o<<<(S * D + 255) / 256, 256, 0, stream>>>(phi, kv, o);
  k_gemm<<<dim3(D / 64, S), 64, 0, stream>>>(o, wo, a, S, D, D);
  k_K<<<dim3(L / 64, D), 64, 0, stream>>>(Are, Aim, Bre, Bim, Cre, Cim, logdt, K);
  k_conv<<<dim3(L / 64, D), 64, 0, stream>>>(x, K, Dsk, b);
  k_final<<<(S * D + 255) / 256, 256, 0, stream>>>(a, b, g, (u16*)d_out);
}
```

```cpp
#include <hip/hip_runtime.h>
#include <stdint.h>

typedef unsigned short u16;
typedef unsigned int u32;
typedef short s16x4 __attribute__((ext_vector_type(4)));
typedef short s16x8 __attribute__((ext_vector_type(8)));
typedef _Float16 f16x8 __attribute__((ext_vector_type(8)));
typedef _Float16 f16x2 __attribute__((ext_vector_type(2)));
typedef __bf16 bf16x8 __attribute__((ext_vector_type(8)));
typedef __bf16 bf16x2 __attribute__((ext_vector_type(2)));
typedef float f32x4 __attribute__((ext_vector_type(4)));
typedef float f32x2 __attribute__((ext_vector_type(2)));
typedef u32 u32x4 __attribute__((ext_vector_type(4)));
typedef u32 u32x2 __attribute__((ext_vector_type(2)));
#define LAS __attribute__((address_space(3)))
#define GAS __attribute__((address_space(1)))
#define DI __device__ __forceinline__

constexpr int S = 2048, D = 448, H = 16, DH = 28, NS = 256;
constexpr float LOG2E = 1.4426950408889634f;
constexpr float INV2PI = 0.15915494309189535f;

constexpr size_t WS_KVP = 0;
constexpr size_t WS_KVF = 2u << 20;
constexpr size_t WS_OF  = 3u << 20;
constexpr size_t WS_YT  = 6u << 20;
constexpr size_t WS_G   = 8u << 20;

struct Params {
  const u16 *x, *wqkv, *wphi, *wo, *Are, *Aim, *Bre, *Bim, *Cre, *Cim, *logdt, *dskip, *gatew;
  u16* out; char* ws;
};

DI float bf2f(u32 v) { return __builtin_bit_cast(float, v << 16); }
DI float bfhi(u32 v) { return __builtin_bit_cast(float, v & 0xffff0000u); }
DI u32 pk_bf16(float a, float b) { f32x2 v = {a, b}; return __builtin_bit_cast(u32, __builtin_convertvector(v, bf16x2)); }
DI u32 pk_f16(float a, float b) { f32x2 v = {a, b}; return __builtin_bit_cast(u32, __builtin_convertvector(v, f16x2)); }
DI float rbf(float a) { return bf2f(pk_bf16(a, 0.f) & 0xffffu); }
DI f32x4 mfma_bf(u32x4 a, u32x4 b, f32x4 c) { return __builtin_amdgcn_mfma_f32_16x16x32_bf16(__builtin_bit_cast(bf16x8, a), __builtin_bit_cast(bf16x8, b), c, 0, 0, 0); }
DI f32x4 mfma_h(u32x4 a, u32x4 b, f32x4 c) { return __builtin_amdgcn_mfma_f32_16x16x32_f16(__builtin_bit_cast(f16x8, a), __builtin_bit_cast(f16x8, b), c, 0, 0, 0); }
DI u32x2 tr_read(LAS char* p) { return __builtin_bit_cast(u32x2, __builtin_amdgcn_ds_read_tr16_b64_v4i16((LAS s16x4*)p)); }
DI u32x4 cat(u32x2 a, u32x2 b) { return (u32x4){a.x, a.y, b.x, b.y}; }
template <int CTRL> DI float dpp(float x) { return __builtin_bit_cast(float, __builtin_amdgcn_mov_dpp(__builtin_bit_cast(int, x), CTRL, 0xf, 0xf, true)); }
DI float row16_max(float x) { x = fmaxf(x, dpp<0xB1>(x)); x = fmaxf(x, dpp<0x4E>(x)); x = fmaxf(x, dpp<0x141>(x)); x = fmaxf(x, dpp<0x140>(x)); return x; }
DI float row16_min(float x) { x = fminf(x, dpp<0xB1>(x)); x = fminf(x, dpp<0x4E>(x)); x = fminf(x, dpp<0x141>(x)); x = fminf(x, dpp<0x140>(x)); return x; }
DI float row16_sum(float x) { x += dpp<0xB1>(x); x += dpp<0x4E>(x); x += dpp<0x141>(x); x += dpp<0x140>(x); return x; }
DI void swap16(float x, float& a, float& b) { const int u = __builtin_bit_cast(int, x); auto r = __builtin_amdgcn_permlane16_swap(u, u, false, false); const int r0 = r[0], r1 = r[1]; a = __builtin_bit_cast(float, r0); b = __builtin_bit_cast(float, r1); }
DI void swap32(float x, float& a, float& b) { const int u = __builtin_bit_cast(int, x); auto r = __builtin_amdgcn_permlane32_swap(u, u, false, false); const int r0 = r[0], r1 = r[1]; a = __builtin_bit_cast(float, r0); b = __builtin_bit_cast(float, r1); }
DI float grp_max(float x) { float a, b; swap16(x, a, b); x = fmaxf(a, b); swap32(x, a, b); return fmaxf(a, b); }
DI float grp_min(float x) { float a, b; swap16(x, a, b); x = fminf(a, b); swap32(x, a, b); return fminf(a, b); }
DI float grp_sum(float x) { float a, b; swap16(x, a, b); x = a + b; swap32(x, a, b); return a + b; }
DI float wave_max(float x) { return grp_max(row16_max(x)); }
DI float wave_sum(float x) { return grp_sum(row16_sum(x)); }
DI float exp2f_(float x) { return __builtin_amdgcn_exp2f(x); }
DI float rcpf_(float x) { return __builtin_amdgcn_rcpf(x); }

struct KIdx { u16 v[2080]; };
constexpr int lb_round(int l) {
  if (l < 256) return l;
  int sh = 0; while ((l >> sh) >= 256) ++sh;
  int step = 1 << sh, base = (l >> sh) << sh, rem = l - base, half = step >> 1;
  if (rem > half || (rem == half && ((l >> sh) & 1))) base += step;
  return base;
}
constexpr int kcompact(int v) {
  if (v < 256) { int i = (v & 7) + 8 * (v >> 7), j = (v >> 3) & 15; return i * 16 + j; }
  if (v < 512) { int o = v - 256; int i = 16 + ((o >> 1) & 3) + 4 * (o >> 7), j = (o >> 3) & 15; return i * 16 + j; }
  if (v < 1024) { int o = v - 512; int i = 24 + ((o >> 2) & 1) + 2 * (o >> 7), j = (o >> 3) & 15; return i * 16 + j; }
  if (v < 2048) { int o = v - 1024; int i = 32 + (o >> 7), j = (o >> 3) & 15; return i * 16 + j; }
  return 40 * 16;
}
constexpr KIdx make_kidx() {
  KIdx t{};
  for (int q = 0; q < 2080; ++q) { int l = 2048 - q; t.v[q] = (l >= 0 && l < 2048) ? (u16)(2 * kcompact(lb_round(l))) : (u16)(2 * 767); }
  return t;
}
__device__ const KIdx g_kidx = make_kidx();

constexpr int WIMG = 448 * 64;
DI void load_wphi(const Params& p, int h, int l16, int g, u32x4 (&wf)[2]) {
#pragma unroll
  for (int c = 0; c < 2; ++c) {
    const int f = 16 * c + l16;
    u32 e[8];
#pragma unroll
    for (int i = 0; i < 8; ++i) {
      const int d = (i < 4) ? 4 * g + i : 16 + 4 * g + (i - 4);
      const bool ok = d < DH && f < DH;
      e[i] = ok ? (u32)p.wphi[(h * DH + d) * DH + f] : 0u;
    }
    wf[c] = (u32x4){e[0] | (e[1] << 16), e[2] | (e[3] << 16), e[4] | (e[5] << 16), e[6] | (e[7] << 16)};
  }
}
DI void dma_w(const Params& p, LAS char* lds, int sect  , int h, int blk0, int nblk, int lane) {
  auto rs = __builtin_amdgcn_make_buffer_rsrc((void*)p.wqkv, 0, D * 3 * D * 2, 0x00020000);
  const int rsub = lane >> 2, ch = lane & 3;
  for (int i = 0; i < nblk; ++i) {
    const int blk = blk0 + i;
    const int row = blk * 16 + rsub;
    const int gofs = (row * (3 * D) + sect * D + h * DH) * 2 + 16 * (ch ^ (((row >> 3) & 1) << 1));
    __builtin_amdgcn_raw_ptr_buffer_load_lds(rs, (LAS void*)(lds + blk * 1024), 16, gofs, 0, 0, 0);
  }
}
DI u32x4 wfrag(LAS char* img, int ks, int c, int lane) {
  const int g = lane >> 4, q = (lane & 15) >> 2, pp = lane & 3;
  LAS char* a = img + (32 * ks + 8 * g + q) * 64 + ((32 * c) ^ ((g & 1) << 5)) + 8 * pp;
  return cat(tr_read(a), tr_read(a + 256));
}

DI void phaseA1(const Params& p, int h, int rr, LAS char* lds) {
  const int tid = threadIdx.x, lane = tid & 63, w = __builtin_amdgcn_readfirstlane(tid >> 6);
  const int l16 = lane & 15, g = lane >> 4;
  dma_w(p, lds + (w >> 1) * WIMG, 1 + (w >> 1), h, (w & 1) * 14, 14, lane);
  const int r0 = rr * 128 + w * 32;
  u32x4 xf[2][14];
#pragma unroll
  for (int rt = 0; rt < 2; ++rt)
#pragma unroll
    for (int ks = 0; ks < 14; ++ks) xf[rt][ks] = *(const u32x4*)(p.x + (size_t)(r0 + 16 * rt + l16) * D + 32 * ks + 8 * g);
  u32x4 wf[2];
  load_wphi(p, h, l16, g, wf);
  asm volatile("s_waitcnt vmcnt(0)" ::: "memory");
  __syncthreads();
  f32x4 ak[2][2], av[2][2];
#pragma unroll
  for (int a = 0; a < 2; ++a)
#pragma unroll
    for (int b = 0; b < 2; ++b) { ak[a][b] = (f32x4){0, 0, 0, 0}; av[a][b] = (f32x4){0, 0, 0, 0}; }
#pragma unroll
  for (int ks = 0; ks < 14; ++ks) {
#pragma unroll
    for (int c = 0; c < 2; ++c) {
      const u32x4 wk = wfrag(lds, ks, c, lane), wv = wfrag(lds + WIMG, ks, c, lane);
#pragma unroll
      for (int rt = 0; rt < 2; ++rt) {
        ak[c][rt] = mfma_bf(wk, xf[rt][ks], ak[c][rt]);
        av[rt][c] = mfma_bf(xf[rt][ks], wv, av[rt][c]);
      }
    }
  }
  float ph[2][2][2][4];
#pragma unroll
  for (int rt = 0; rt < 2; ++rt) {
    u32x4 kf = {pk_bf16(ak[0][rt][0], ak[0][rt][1]), pk_bf16(ak[0][rt][2], ak[0][rt][3]), pk_bf16(ak[1][rt][0], ak[1][rt][1]), pk_bf16(ak[1][rt][2], ak[1][rt][3])};
    if (g == 3) { kf[2] = 0; kf[3] = 0; }
    const f32x4 z4 = {0, 0, 0, 0};
    const f32x4 p0 = mfma_bf(kf, wf[0], z4), p1 = mfma_bf(kf, wf[1], z4);
    const bool valid1 = l16 < 12;
#pragma unroll
    for (int r = 0; r < 4; ++r) {
      const float a = rbf(p0[r]) * LOG2E, b = rbf(p1[r]) * LOG2E;
      const float mx = row16_max(valid1 ? fmaxf(a, b) : a), mn = row16_min(valid1 ? fminf(a, b) : a);
      const float ea = exp2f_(a - mx), eb = valid1 ? exp2f_(b - mx) : 0.f;
      const float na = exp2f_(mn - a), nb = valid1 ? exp2f_(mn - b) : 0.f;
      const float ip = rcpf_(row16_sum(ea + eb)), in = rcpf_(row16_sum(na + nb));
      ph[0][0][rt][r] = ea * ip; ph[0][1][rt][r] = eb * ip; ph[1][0][rt][r] = na * in; ph[1][1][rt][r] = nb * in;
    }
  }
  u32x4 vb[2];
#pragma unroll
  for (int c = 0; c < 2; ++c) vb[c] = (u32x4){pk_bf16(av[0][c][0], av[0][c][1]), pk_bf16(av[0][c][2], av[0][c][3]), pk_bf16(av[1][c][0], av[1][c][1]), pk_bf16(av[1][c][2], av[1][c][3])};
  if (l16 == 12) vb[1] = (u32x4){0x3f803f80u, 0x3f803f80u, 0x3f803f80u, 0x3f803f80u};
  __syncthreads();
  LAS f32x4* red = (LAS f32x4*)lds;
#pragma unroll
  for (int sg = 0; sg < 2; ++sg)
#pragma unroll
    for (int cp = 0; cp < 2; ++cp) {
      const u32x4 pa = {pk_bf16(ph[sg][cp][0][0], ph[sg][cp][0][1]), pk_bf16(ph[sg][cp][0][2], ph[sg][cp][0][3]), pk_bf16(ph[sg][cp][1][0], ph[sg][cp][1][1]), pk_bf16(ph[sg][cp][1][2], ph[sg][cp][1][3])};
#pragma unroll
      for (int c = 0; c < 2; ++c) {
        const f32x4 z4 = {0, 0, 0, 0};
        red[(w * 8 + (sg * 2 + cp) * 2 + c) * 64 + lane] = mfma_bf(pa, vb[c], z4);
      }
    }
  __syncthreads();
  f32x4* dst = (f32x4*)(p.ws + WS_KVP) + (size_t)(h * 16 + rr) * 512;
#pragma unroll
  for (int i = 0; i < 2; ++i) {
    const int e = tid + 256 * i;
    const f32x4 s = (red[e] + red[512 + e]) + (red[1024 + e] + red[1536 + e]);
    dst[e] = s;
  }
}

__global__ void __launch_bounds__(128) k_red(Params p) {
  const int h = blockIdx.x >> 1, sg = blockIdx.x & 1;
  const int c = threadIdx.x >> 6, lane = threadIdx.x & 63;
  const f32x4* src = (const f32x4*)(p.ws + WS_KVP) + (size_t)h * 16 * 512;
  f32x4 s0 = {0, 0, 0, 0}, s1 = {0, 0, 0, 0};
#pragma unroll
  for (int rr = 0; rr < 16; ++rr) {
    s0 += src[rr * 512 + ((sg * 2 + 0) * 2 + c) * 64 + lane];
    s1 += src[rr * 512 + ((sg * 2 + 1) * 2 + c) * 64 + lane];
  }
  u32x4 o = {pk_bf16(s0[0], s0[1]), pk_bf16(s0[2], s0[3]), pk_bf16(s1[0], s1[1]), pk_bf16(s1[2], s1[3])};
  ((u32x4*)(p.ws + WS_KVF))[((h * 2 + sg) * 2 + c) * 64 + lane] = o;
}

DI void phaseA2(const Params& p, int h, int rr, LAS char* lds) {
  const int tid = threadIdx.x, lane = tid & 63, w = __builtin_amdgcn_readfirstlane(tid >> 6);
  const int l16 = lane & 15, g = lane >> 4;
  dma_w(p, lds, 0, h, w * 7, 7, lane);
  const int r0 = rr * 128 + w * 32;
  u32x4 xf[2][14];
#pragma unroll
  for (int rt = 0; rt < 2; ++rt)
#pragma unroll
    for (int ks = 0; ks < 14; ++ks) xf[rt][ks] = *(const u32x4*)(p.x + (size_t)(r0 + 16 * rt + l16) * D + 32 * ks + 8 * g);
  u32x4 wf[2];
  load_wphi(p, h, l16, g, wf);
  u32x4 kvf[2][2];
#pragma unroll
  for (int sg = 0; sg < 2; ++sg)
#pragma unroll
    for (int c = 0; c < 2; ++c) kvf[sg][c] = ((const u32x4*)(p.ws + WS_KVF))[((h * 2 + sg) * 2 + c) * 64 + lane];
  asm volatile("s_waitcnt vmcnt(0)" ::: "memory");
  __syncthreads();
  for (int k = tid; k < D; k += 256) *(LAS u16*)(lds + k * 64 + 16 * (3 ^ (((k >> 3) & 1) << 1)) + 10) = p.gatew[k];
  __syncthreads();
  f32x4 aq[2][2];
#pragma unroll
  for (int a = 0; a < 2; ++a)
#pragma unroll
    for (int b = 0; b < 2; ++b) aq[a][b] = (f32x4){0, 0, 0, 0};
#pragma unroll
  for (int ks = 0; ks < 14; ++ks) {
#pragma unroll
    for (int c = 0; c < 2; ++c) {
      const u32x4 wq = wfrag(lds, ks, c, lane);
#pragma unroll
      for (int rt = 0; rt < 2; ++rt) aq[c][rt] = mfma_bf(wq, xf[rt][ks], aq[c][rt]);
    }
  }
#pragma unroll
  for (int rt = 0; rt < 2; ++rt) {
    if (h == 0 && g == 3) {
      const float lg = rbf(aq[1][rt][1]);
      ((float*)(p.ws + WS_G))[r0 + 16 * rt + l16] = rbf(rcpf_(1.f + exp2f_(-lg * LOG2E)));
    }
    u32x4 qf = {pk_bf16(aq[0][rt][0], aq[0][rt][1]), pk_bf16(aq[0][rt][2], aq[0][rt][3]), pk_bf16(aq[1][rt][0], aq[1][rt][1]), pk_bf16(aq[1][rt][2], aq[1][rt][3])};
    if (g == 3) { qf[2] = 0; qf[3] = 0; }
    const f32x4 z4 = {0, 0, 0, 0};
    const f32x4 p0 = mfma_bf(wf[0], qf, z4), p1 = mfma_bf(wf[1], qf, z4);
    float a[4], b[4];
    const bool valid1 = g < 3;
#pragma unroll
    for (int r = 0; r < 4; ++r) { a[r] = rbf(p0[r]) * LOG2E; b[r] = rbf(p1[r]) * LOG2E; }
    float mx = fmaxf(fmaxf(a[0], a[1]), fmaxf(a[2], a[3])), mn = fminf(fminf(a[0], a[1]), fminf(a[2], a[3]));
    if (valid1) { mx = fmaxf(mx, fmaxf(fmaxf(b[0], b[1]), fmaxf(b[2], b[3]))); mn = fminf(mn, fminf(fminf(b[0], b[1]), fminf(b[2], b[3]))); }
    mx = grp_max(mx); mn = grp_min(mn);
    float ea[4], eb[4], na[4], nb[4], sp = 0.f, sn = 0.f;
#pragma unroll
    for (int r = 0; r < 4; ++r) {
      ea[r] = exp2f_(a[r] - mx); eb[r] = valid1 ? exp2f_(b[r] - mx) : 0.f;
      na[r] = exp2f_(mn - a[r]); nb[r] = valid1 ? exp2f_(mn - b[r]) : 0.f;
      sp += ea[r] + eb[r]; sn += na[r] + nb[r];
    }
    const float ip = rcpf_(grp_sum(sp)), in = rcpf_(grp_sum(sn));
    const u32x4 fp = {pk_bf16(ea[0] * ip, ea[1] * ip), pk_bf16(ea[2] * ip, ea[3] * ip), pk_bf16(eb[0] * ip, eb[1] * ip), pk_bf16(eb[2] * ip, eb[3] * ip)};
    const u32x4 fn = {pk_bf16(na[0] * in, na[1] * in), pk_bf16(na[2] * in, na[3] * in), pk_bf16(nb[0] * in, nb[1] * in), pk_bf16(nb[2] * in, nb[3] * in)};
    f32x4 n0 = mfma_bf(kvf[0][0], fp, z4), n1 = mfma_bf(kvf[0][1], fp, z4);
    n0 = mfma_bf(kvf[1][0], fn, n0); n1 = mfma_bf(kvf[1][1], fn, n1);
    float t0, t1;
    swap32(n1[0], t0, t1);
    swap16(t1, t0, t1);
    const float den = rbf(rbf(t1) + 1e-6f);
    const float id = rcpf_(den);
    u32x4 of = {pk_bf16(rbf(n0[0]) * id, rbf(n0[1]) * id), pk_bf16(rbf(n0[2]) * id, rbf(n0[3]) * id), pk_bf16(rbf(n1[0]) * id, rbf(n1[1]) * id), pk_bf16(rbf(n1[2]) * id, rbf(n1[3]) * id)};
    if (g == 3) { of[2] = 0; of[3] = 0; }
    ((u32x4*)(p.ws + WS_OF))[(size_t)(((r0 >> 4) + rt) * 16 + h) * 64 + lane] = of;
  }
}

DI void phaseF(const Params& p, int h, int rr, LAS char* lds) {
  const int tid = threadIdx.x, lane = tid & 63, w = __builtin_amdgcn_readfirstlane(tid >> 6);
  const int l16 = lane & 15, g = lane >> 4;
  const int R0 = rr * 128 + 64 * (h & 1) + 16 * w, C0 = 56 * (h >> 1);
  {
    auto rs = __builtin_amdgcn_make_buffer_rsrc((void*)p.wo, 0, D * D * 2, 0x00020000);
    const int rsub = lane >> 3, pos = lane & 7;
    for (int blk = w; blk < 57; blk += 4) {
      const int row = blk * 8 + rsub;
      const int ch = (((pos >> 1) ^ ((row >> 1) & 3)) << 1) | (pos & 1);
      const int gofs = row < D ? (row * D + C0) * 2 + 16 * ch : 0x7ffffff0;
      __builtin_amdgcn_raw_ptr_buffer_load_lds(rs, (LAS void*)(lds + blk * 1024), 16, gofs, 0, 0, 0);
    }
  }
  u32x4 of[16];
#pragma unroll
  for (int hh = 0; hh < 16; ++hh) of[hh] = ((const u32x4*)(p.ws + WS_OF))[(size_t)((R0 >> 4) * 16 + hh) * 64 + lane];
  const int l = R0 + l16;
  const float gg = ((const float*)(p.ws + WS_G))[l];
  u32 yv[4][4]; u32x2 xv[4], dv[4];
  const u16* yT = (const u16*)(p.ws + WS_YT);
#pragma unroll
  for (int ct = 0; ct < 4; ++ct) {
    const int cc = C0 + 16 * ct + 4 * g;
    const bool ok = 16 * ct + 4 * g < 56;
    const int cs = ok ? cc : C0;
#pragma unroll
    for (int r = 0; r < 4; ++r) yv[ct][r] = yT[(size_t)(cs + r) * S + l];
    xv[ct] = *(const u32x2*)(p.x + (size_t)l * D + cs);
    dv[ct] = *(const u32x2*)(p.dskip + cs);
  }
  asm volatile("s_waitcnt vmcnt(0)" ::: "memory");
  __syncthreads();
  f32x4 acc[4];
#pragma unroll
  for (int ct = 0; ct < 4; ++ct) acc[ct] = (f32x4){0, 0, 0, 0};
  const int q = l16 >> 2, pp = lane & 3;
#pragma unroll
  for (int hh = 0; hh < 16; ++hh) {
    const int rowa = 28 * hh + 4 * g + q, rowb = rowa + 16;
#pragma unroll
    for (int ct = 0; ct < 4; ++ct) {
      const u32x2 lo = tr_read(lds + rowa * 128 + ((ct ^ ((rowa >> 1) & 3)) << 5) + 8 * pp);
      const u32x2 hi = tr_read(lds + rowb * 128 + ((ct ^ ((rowb >> 1) & 3)) << 5) + 8 * pp);
      acc[ct] = mfma_bf(cat(lo, hi), of[hh], acc[ct]);
    }
  }
  const float g1 = rbf(1.f - gg);
#pragma unroll
  for (int ct = 0; ct < 4; ++ct) {
    if (16 * ct + 4 * g < 56) {
      float o[4];
      const float xs[4] = {bf2f(xv[ct].x & 0xffffu), bfhi(xv[ct].x), bf2f(xv[ct].y & 0xffffu), bfhi(xv[ct].y)};
      const float ds[4] = {bf2f(dv[ct].x & 0xffffu), bfhi(dv[ct].x), bf2f(dv[ct].y & 0xffffu), bfhi(dv[ct].y)};
#pragma unroll
      for (int r = 0; r < 4; ++r) {
        const float a = rbf(acc[ct][r]);
        const float b = rbf(bf2f(yv[ct][r]) + rbf(xs[r] * ds[r]));
        o[r] = rbf(gg * a) + rbf(g1 * b);
      }
      *(u32x2*)(p.out + (size_t)l * D + C0 + 16 * ct + 4 * g) = (u32x2){pk_bf16(o[0], o[1]), pk_bf16(o[2], o[3])};
    }
  }
}

constexpr int KCOPY_CH = 33040;
constexpr int LB_U = 2 * KCOPY_CH;
constexpr int LB_KC = LB_U + 2 * 4608;
constexpr int LB_MISC = LB_KC + 2 * 1536;
constexpr int LB_STG = 0;
constexpr int LB_KT = 4 * 8448;
constexpr int LDS_BYTES = 80 * 1024;
__device__ constexpr int KBASE[8] = {258, 1807, 1290, 0, 774, 516, 1032, 1548};

struct cpx2 { f32x2 re, im; };
DI cpx2 cmul(const cpx2& a, const cpx2& b) { cpx2 r; r.re = a.re * b.re - a.im * b.im; r.im = a.re * b.im + a.im * b.re; return r; }
DI cpx2 csqr(const cpx2& a) { cpx2 r; r.re = a.re * a.re - a.im * a.im; r.im = (a.re + a.re) * a.im; return r; }
DI cpx2 cexp_m(f32x2 al, f32x2 th, float m) {
  cpx2 r;
#pragma unroll
  for (int i = 0; i < 2; ++i) {
    const float mag = exp2f_(al[i] * m);
    const float ph = __builtin_amdgcn_fractf(th[i] * m);
    r.re[i] = mag * __builtin_amdgcn_cosf(ph); r.im[i] = mag * __builtin_amdgcn_sinf(ph);
  }
  return r;
}
DI void put_row(LAS char* stg, int i, int lane, const cpx2& v) { *(LAS u32x2*)(stg + i * 528 + lane * 8) = (u32x2){pk_f16(v.re[0], v.im[0]), pk_f16(v.re[1], v.im[1])}; }
DI void put_rowc(LAS char* stg, int i, int lane, const cpx2& v) { *(LAS u32x2*)(stg + i * 528 + lane * 8) = (u32x2){pk_f16(v.re[0], -v.im[0]), pk_f16(v.re[1], -v.im[1])}; }

template <int ROLE> DI void conv_role(LAS char* abase, LAS char* bbase, u16* ydst, float inv, int lane) {
  constexpr int NCB = ROLE == 0 ? 3 : 5;
  constexpr int CB[5] = {ROLE == 0 ? 7 : 5, ROLE == 0 ? 6 : 4, ROLE == 0 ? 2 : 3, 1, 0};
  constexpr int MAXI = ROLE == 0 ? 8 : 6;
  f32x4 acc[NCB];
#pragma unroll
  for (int i = 0; i < NCB; ++i) acc[i] = (f32x4){0, 0, 0, 0};
#pragma unroll 1
  for (int it = 0; it < 8; ++it) {
    LAS char* a = abase - 64 * it;
    LAS char* b = bbase - 64 * it;
    u32x4 A[MAXI], B[MAXI];
#pragma unroll
    for (int s = 0; s < MAXI; ++s) { A[s] = *(LAS u32x4*)(a + 512 * (7 - s)); B[s] = *(LAS u32x4*)(b + 512 * s); }
#pragma unroll
    for (int i = 0; i < NCB; ++i)
#pragma unroll
      for (int s = 0; s <= CB[i]; ++s) acc[i] = mfma_h(A[s], B[CB[i] - s], acc[i]);
  }
  const int l16 = lane & 15, g = lane >> 4;
#pragma unroll
  for (int i = 0; i < NCB; ++i)
    *(u32x2*)(ydst + 256 * CB[i] + 16 * l16 + 4 * g) = (u32x2){pk_bf16(acc[i][0] * inv, acc[i][1] * inv), pk_bf16(acc[i][2] * inv, acc[i][3] * inv)};
}

DI float pow2_scale(float mx, int target) {
  const int e = (int)((__builtin_bit_cast(u32, mx) >> 23) & 0xff);
  int se = 127 + target - (e - 127);
  se = se < 1 ? 1 : (se > 254 ? 254 : se);
  return __builtin_bit_cast(float, (u32)se << 23);
}

DI void phaseB(const Params& p, int d0, LAS char* lds) {
  const int tid = threadIdx.x, lane = tid & 63, w = __builtin_amdgcn_readfirstlane(tid >> 6);
  const int l16 = lane & 15, g = lane >> 4;
  const int ch = w >> 1, half = w & 1, d = d0 + ch;
  LAS float* misc = (LAS float*)(lds + LB_MISC);
  u32 uv[8];
#pragma unroll
  for (int i = 0; i < 8; ++i) uv[i] = *(const u32*)(p.x + (size_t)(tid + 256 * i) * D + d0);
  const int pi = (d * NS + half * 128 + 2 * lane) >> 1;
  const u32 are = ((const u32*)p.Are)[pi], aim = ((const u32*)p.Aim)[pi], bre = ((const u32*)p.Bre)[pi], bim = ((const u32*)p.Bim)[pi], cre = ((const u32*)p.Cre)[pi], cim = ((const u32*)p.Cim)[pi];
  const float dt = rbf(__expf(bf2f(p.logdt[d])));
  const u32x4 kix0 = *(const u32x4*)(g_kidx.v + 8 * tid), kix1 = *(const u32x4*)(g_kidx.v + 8 * tid + 8);
  const f32x2 al = (f32x2){bf2f(are & 0xffffu), bfhi(are)} * (dt * LOG2E);
  const f32x2 th = (f32x2){bf2f(aim & 0xffffu), bfhi(aim)} * dt * INV2PI;
  const f32x2 br = (f32x2){bf2f(bre & 0xffffu), bfhi(bre)} * dt, bi = (f32x2){bf2f(bim & 0xffffu), bfhi(bim)} * dt;
  const f32x2 cr = {bf2f(cre & 0xffffu), bfhi(cre)}, ci = {bf2f(cim & 0xffffu), bfhi(cim)};
  cpx2 W; W.re = cr * br - ci * bi; W.im = cr * bi + ci * br;
  float wm0 = __builtin_amdgcn_sqrtf(W.re[0] * W.re[0] + W.im[0] * W.im[0]) * exp2f_(fmaxf(0.f, al[0] * 2048.f));
  float wm1 = __builtin_amdgcn_sqrtf(W.re[1] * W.re[1] + W.im[1] * W.im[1]) * exp2f_(fmaxf(0.f, al[1] * 2048.f));
  const float wmax = wave_max(fmaxf(wm0, wm1)), wsum = wave_sum(wm0 + wm1);
  const float SR = pow2_scale(wmax, 14);
  W.re *= SR; W.im *= SR;
  LAS char* stg = lds + LB_STG + w * 8448;
  const cpx2 Z8 = cexp_m(al, th, 8.f);
  const cpx2 Z16 = csqr(Z8), Z32 = csqr(Z16), Z64 = csqr(Z32);
  {
    cpx2 C[8];
    C[0].re = (f32x2){1.f, 1.f}; C[0].im = (f32x2){0.f, 0.f};
    C[1] = Z8; C[2] = Z16; C[3] = cmul(Z8, Z16); C[4] = Z32; C[5] = cmul(Z32, Z8); C[6] = cmul(Z32, Z16); C[7] = cmul(Z32, C[3]);
#pragma unroll
    for (int j = 0; j < 8; ++j) put_rowc(stg, j, lane, C[j]);
    put_rowc(stg, 8, lane, Z64);
#pragma unroll
    for (int j = 1; j < 8; ++j) put_rowc(stg, 8 + j, lane, cmul(Z64, C[j]));
  }
  u32x4 Bf[8];
#pragma unroll
  for (int ks = 0; ks < 8; ++ks) Bf[ks] = *(LAS u32x4*)(stg + l16 * 528 + 64 * ks + 16 * g);
  const cpx2 Z1 = cexp_m(al, th, 1.f);
  const cpx2 Z2 = csqr(Z1), Z4 = csqr(Z2);
  cpx2 Sm[8];
  Sm[1] = Z1; Sm[2] = Z2; Sm[3] = cmul(Z1, Z2); Sm[4] = Z4; Sm[5] = cmul(Z4, Z1); Sm[6] = cmul(Z4, Z2); Sm[7] = cmul(Z4, Sm[3]);
  const cpx2 Z128 = cexp_m(al, th, 128.f);
  const cpx2 Z256 = csqr(Z128), Z512 = csqr(Z256);
  const cpx2 Z1024 = cexp_m(al, th, 1024.f);
  const cpx2 Z2048 = csqr(Z1024);
  f32x4 kacc[3];
  {
    const cpx2 B1 = cmul(W, Z128);
    put_row(stg, 0, lane, W);
#pragma unroll
    for (int i = 1; i < 8; ++i) put_row(stg, i, lane, cmul(W, Sm[i]));
    put_row(stg, 8, lane, B1);
#pragma unroll
    for (int i = 1; i < 8; ++i) put_row(stg, 8 + i, lane, cmul(B1, Sm[i]));
    f32x4 acc = {0, 0, 0, 0};
#pragma unroll
    for (int ks = 0; ks < 8; ++ks) acc = mfma_h(*(LAS u32x4*)(stg + l16 * 528 + 64 * ks + 16 * g), Bf[ks], acc);
    kacc[0] = acc;
  }
  const cpx2 Z384 = cmul(Z256, Z128);
  {
    const cpx2 B2 = cmul(W, Z256), B3 = cmul(W, Z384), B4 = cmul(W, Z512);
    const cpx2 B5 = cmul(B4, Z128), B6 = cmul(B4, Z256), B7 = cmul(B4, Z384);
    put_row(stg, 0, lane, B2); put_row(stg, 1, lane, cmul(B2, Sm[2])); put_row(stg, 2, lane, cmul(B2, Sm[4])); put_row(stg, 3, lane, cmul(B2, Sm[6]));
    put_row(stg, 4, lane, B3); put_row(stg, 5, lane, cmul(B3, Sm[2])); put_row(stg, 6, lane, cmul(B3, Sm[4])); put_row(stg, 7, lane, cmul(B3, Sm[6]));
    put_row(stg, 8, lane, B4); put_row(stg, 9, lane, cmul(B4, Sm[4])); put_row(stg, 10, lane, B5); put_row(stg, 11, lane, cmul(B5, Sm[4]));
    put_row(stg, 12, lane, B6); put_row(stg, 13, lane, cmul(B6, Sm[4])); put_row(stg, 14, lane, B7); put_row(stg, 15, lane, cmul(B7, Sm[4]));
    f32x4 acc = {0, 0, 0, 0};
#pragma unroll
    for (int ks = 0; ks < 8; ++ks) acc = mfma_h(*(LAS u32x4*)(stg + l16 * 528 + 64 * ks + 16 * g), Bf[ks], acc);
    kacc[1] = acc;
  }
  {
    const cpx2 B8 = cmul(W, Z1024);
    const cpx2 Z640 = cmul(Z512, Z128), Z768 = cmul(Z512, Z256), Z896 = cmul(Z512, Z384);
    put_row(stg, 0, lane, B8); put_row(stg, 1, lane, cmul(B8, Z128)); put_row(stg, 2, lane, cmul(B8, Z256)); put_row(stg, 3, lane, cmul(B8, Z384));
    put_row(stg, 4, lane, cmul(B8, Z512)); put_row(stg, 5, lane, cmul(B8, Z640)); put_row(stg, 6, lane, cmul(B8, Z768)); put_row(stg, 7, lane, cmul(B8, Z896));
    put_row(stg, 8, lane, cmul(W, Z2048));
    f32x4 acc = {0, 0, 0, 0};
#pragma unroll
    for (int ks = 0; ks < 8; ++ks) acc = mfma_h(*(LAS u32x4*)(stg + l16 * 528 + 64 * ks + 16 * g), Bf[ks], acc);
    kacc[2] = acc;
  }
  {
    LAS float* kt = (LAS float*)(lds + LB_KT) + (ch * 2 + half) * 768;
    const float isr = rcpf_(SR);
#pragma unroll
    for (int t = 0; t < 3; ++t)
#pragma unroll
      for (int r = 0; r < 4; ++r) kt[(16 * t + 4 * g + r) * 16 + l16] = kacc[t][r] * isr;
    if (lane == 0) misc[w] = wsum;
  }
  {
    float m0 = 0.f, m1 = 0.f;
#pragma unroll
    for (int i = 0; i < 8; ++i) { m0 = fmaxf(m0, fabsf(bf2f(uv[i] & 0xffffu))); m1 = fmaxf(m1, fabsf(bfhi(uv[i]))); }
    m0 = wave_max(m0); m1 = wave_max(m1);
    if (lane == 0) { misc[4 + w] = m0; misc[8 + w] = m1; }
  }
  __syncthreads();
  const float SK0 = pow2_scale(misc[0] + misc[1], 14), SK1 = pow2_scale(misc[2] + misc[3], 14);
  const float SU0 = pow2_scale(fmaxf(fmaxf(misc[4], misc[5]), fmaxf(misc[6], misc[7])), 14);
  const float SU1 = pow2_scale(fmaxf(fmaxf(misc[8], misc[9]), fmaxf(misc[10], misc[11])), 14);
  {
    LAS float* kt = (LAS float*)(lds + LB_KT);
#pragma unroll
    for (int i = 0; i < 6; ++i) {
      const int e = tid + 256 * i, c = e >= 768, idx = e - 768 * c;
      float v = (kt[(c * 2) * 768 + idx] + kt[(c * 2 + 1) * 768 + idx]) * (c ? SK1 : SK0);
      if (idx >= 41 * 16) v = 0.f;
      *(LAS _Float16*)(lds + LB_KC + c * 1536 + idx * 2) = (_Float16)v;
    }
#pragma unroll
    for (int i = 0; i < 8; ++i) {
      *(LAS _Float16*)(lds + LB_U + (256 + tid + 256 * i) * 2) = (_Float16)(bf2f(uv[i] & 0xffffu) * SU0);
      *(LAS _Float16*)(lds + LB_U + 4608 + (256 + tid + 256 * i) * 2) = (_Float16)(bfhi(uv[i]) * SU1);
    }
    if (tid < 128) { *(LAS u32*)(lds + LB_U + tid * 4) = 0; *(LAS u32*)(lds + LB_U + 4608 + tid * 4) = 0; }
  }
  __syncthreads();
#pragma unroll
  for (int c = 0; c < 2; ++c) {
    LAS char* kc = lds + LB_KC + c * 1536;
    LAS char* cp = lds + c * KCOPY_CH;
    for (int pass = 0; pass < 2; ++pass) {
      if (pass == 1 && tid >= 2) break;
      u32x4 i0 = kix0, i1 = kix1; int m = tid;
      if (pass == 1) { m = 256 + tid; i0 = *(const u32x4*)(g_kidx.v + 8 * m); i1 = *(const u32x4*)(g_kidx.v + 8 * m + 8); }
      u32 dw[8];
#pragma unroll
      for (int k = 0; k < 4; ++k) {
        dw[k] = (u32)*(LAS u16*)(kc + (i0[k] & 0xffffu)) | ((u32)*(LAS u16*)(kc + (i0[k] >> 16)) << 16);
        dw[4 + k] = (u32)*(LAS u16*)(kc + (i1[k] & 0xffffu)) | ((u32)*(LAS u16*)(kc + (i1[k] >> 16)) << 16);
      }
      u32 od[7];
#pragma unroll
      for (int k = 0; k < 7; ++k) od[k] = __builtin_amdgcn_alignbit(dw[k + 1], dw[k], 16);
#pragma unroll
      for (int s = 0; s < 4; ++s) {
        *(LAS u32x4*)(cp + (KBASE[2 * s] + m) * 16) = (u32x4){dw[s], dw[s + 1], dw[s + 2], dw[s + 3]};
        *(LAS u32x4*)(cp + (KBASE[2 * s + 1] + m) * 16) = (u32x4){od[s], od[s + 1], od[s + 2], od[s + 3]};
      }
    }
  }
  __syncthreads();
  {
    const int cc = (8 - (l16 & 7)) & 7;
    int kb = 0;
#pragma unroll
    for (int i = 0; i < 8; ++i) if (cc == i) kb = KBASE[i];
    const int m113 = (2048 - 16 * 113 - l16 + 8 * g - cc) >> 3;
    LAS char* abase = lds + ch * KCOPY_CH + (kb + m113) * 16;
    LAS char* bbase = lds + LB_U + ch * 4608 + (256 + 16 * (l16 - 1) + 8 * g) * 2;
    const float inv = rcpf_((ch ? SK1 : SK0) * (ch ? SU1 : SU0));
    u16* ydst = (u16*)(p.ws + WS_YT) + (size_t)d * S;
    if (half == 0) conv_role<0>(abase, bbase, ydst, inv, lane); else conv_role<1>(abase, bbase, ydst, inv, lane);
  }
}

DI void map_a(int b, int& h, int& rr) { const int xcd = b & 7, j = b >> 3; h = 8 * (xcd & 1) + (j & 7); rr = 4 * (xcd >> 1) + (j >> 3); }

__global__ void __launch_bounds__(256, 2) k_p1(Params p) {
  extern __shared__ __attribute__((aligned(16))) char smem[];
  LAS char* lds = (LAS char*)smem;
  const int b = blockIdx.x;
  if (b < 256) { int h, rr; map_a(b, h, rr); phaseA1(p, h, rr, lds); }
  else { const int bb = b - 256; phaseB(p, 2 * ((bb & 7) * 28 + (bb >> 3)), lds); }
}
__global__ void __launch_bounds__(256, 2) k_a2(Params p) {
  extern __shared__ __attribute__((aligned(16))) char smem[];
  int h, rr; map_a(blockIdx.x, h, rr); phaseA2(p, h, rr, (LAS char*)smem);
}
__global__ void __launch_bounds__(256, 2) k_fin(Params p) {
  extern __shared__ __attribute__((aligned(16))) char smem[];
  int h, rr; map_a(blockIdx.x, h, rr); phaseF(p, h, rr, (LAS char*)smem);
}

extern "C" void kernel_launch(void* const* d_in, const int* in_sizes, int n_in, void* d_out, int out_size, void* d_ws, size_t ws_size, hipStream_t stream) {
  Params p;
  p.x = (const u16*)d_in[0]; p.wqkv = (const u16*)d_in[1]; p.wphi = (const u16*)d_in[2]; p.wo = (const u16*)d_in[3];
  p.Are = (const u16*)d_in[5]; p.Aim = (const u16*)d_in[6]; p.Bre = (const u16*)d_in[7]; p.Bim = (const u16*)d_in[8];
  p.Cre = (const u16*)d_in[9]; p.Cim = (const u16*)d_in[10]; p.logdt = (const u16*)d_in[11]; p.dskip = (const u16*)d_in[12]; p.gatew = (const u16*)d_in[13];
  p.out = (u16*)d_out; p.ws = (char*)d_ws;
  (void)hipFuncSetAttribute((const void*)k_p1, hipFuncAttributeMaxDynamicSharedMemorySize, LDS_BYTES);
  (void)hipFuncSetAttribute((const void*)k_a2, hipFuncAttributeMaxDynamicSharedMemorySize, LDS_BYTES);
  (void)hipFuncSetAttribute((const void*)k_fin, hipFuncAttributeMaxDynamicSharedMemorySize, LDS_BYTES);
  k_p1<<<480, 256, LDS_BYTES, stream>>>(p);
  k_red<<<32, 128, 0, stream>>>(p);
  k_a2<<<256, 256, LDS_BYTES, stream>>>(p);
  k_fin<<<256, 256, LDS_BYTES, stream>>>(p);
}
```

```cpp
#include <hip/hip_runtime.h>
#include <stdint.h>

typedef unsigned short u16;
typedef unsigned int u32;
typedef unsigned long long u64;
typedef short s16x4 __attribute__((ext_vector_type(4)));
typedef _Float16 f16x8 __attribute__((ext_vector_type(8)));
typedef _Float16 f16x2 __attribute__((ext_vector_type(2)));
typedef _Float16 f16x4 __attribute__((ext_vector_type(4)));
typedef __bf16 bf16x8 __attribute__((ext_vector_type(8)));
typedef __bf16 bf16x2 __attribute__((ext_vector_type(2)));
typedef float f32x16 __attribute__((ext_vector_type(16)));
typedef float f32x4 __attribute__((ext_vector_type(4)));
typedef float f32x2 __attribute__((ext_vector_type(2)));
typedef u32 u32x4 __attribute__((ext_vector_type(4)));
typedef u32 u32x2 __attribute__((ext_vector_type(2)));
typedef u16 u16x2 __attribute__((ext_vector_type(2)));
#define LAS __attribute__((address_space(3)))
#define GAS __attribute__((address_space(1)))
#define DI __device__ __forceinline__
typedef __amdgpu_buffer_rsrc_t rsrc_t;

constexpr int S = 2048, D = 448, H = 16, DH = 28, NS = 256;
constexpr float LOG2E = 1.4426950408889634f;
constexpr float INV2PI = 0.15915494309189535f;

constexpr u32 WS_EPOCH = 0;
constexpr u32 WS_TMO   = 256;
constexpr u32 WS_SEEN  = 4096;
constexpr u32 WS_XFLAG = 8192;
constexpr u32 WS_G     = 16384;
constexpr u32 WS_G2    = 64u << 10;
constexpr u32 WS_G1    = 1u << 20;
constexpr u32 WS_OF    = 5u << 20;
constexpr u32 WS_XT    = 7u << 20;
constexpr u32 WS_YT    = 9u << 20;
constexpr u32 WS_ST    = 12u << 20;
constexpr u32 WS_USED  = 16u << 20;

#define STAMP(kid, idx)

struct Params {
  const u16 *x, *wqkv, *wphi, *wo, *Are, *Aim, *Bre, *Bim, *Cre, *Cim, *logdt, *dskip, *gatew;
  u16* out; char* ws;
};

DI u32 f2u(float v) { return __builtin_bit_cast(u32, v); }
DI float u2f(u32 v) { return __builtin_bit_cast(float, v); }
DI float bf2f(u32 v) { return __builtin_bit_cast(float, v << 16); }
DI float bfhi(u32 v) { return __builtin_bit_cast(float, v & 0xffff0000u); }
DI u32 pk_bf16(float a, float b) { f32x2 v = {a, b}; return __builtin_bit_cast(u32, __builtin_convertvector(v, bf16x2)); }
DI u32 pk_f16(float a, float b) { f32x2 v = {a, b}; return __builtin_bit_cast(u32, __builtin_convertvector(v, f16x2)); }
DI float rbf(float a) { return bf2f(pk_bf16(a, 0.f)); }
DI f32x4 mfma16(u32x4 a, u32x4 b, f32x4 c) { return __builtin_amdgcn_mfma_f32_16x16x32_bf16(__builtin_bit_cast(bf16x8, a), __builtin_bit_cast(bf16x8, b), c, 0, 0, 0); }
DI f32x16 mfma32(u32x4 a, u32x4 b, f32x16 c) { return __builtin_amdgcn_mfma_f32_32x32x16_bf16(__builtin_bit_cast(bf16x8, a), __builtin_bit_cast(bf16x8, b), c, 0, 0, 0); }
DI f32x4 mfma_h(u32x4 a, u32x4 b, f32x4 c) { return __builtin_amdgcn_mfma_f32_16x16x32_f16(__builtin_bit_cast(f16x8, a), __builtin_bit_cast(f16x8, b), c, 0, 0, 0); }
DI u32x2 tr_read(LAS char* p) { return __builtin_bit_cast(u32x2, __builtin_amdgcn_ds_read_tr16_b64_v4i16((LAS s16x4*)p)); }
DI u32x4 cat(u32x2 a, u32x2 b) { return (u32x4){a.x, a.y, b.x, b.y}; }
template <int CTRL> DI float dpp(float x) { return __builtin_bit_cast(float, __builtin_amdgcn_mov_dpp(__builtin_bit_cast(int, x), CTRL, 0xf, 0xf, true)); }
DI float row16_max(float x) { x = fmaxf(x, dpp<0xB1>(x)); x = fmaxf(x, dpp<0x4E>(x)); x = fmaxf(x, dpp<0x141>(x)); x = fmaxf(x, dpp<0x140>(x)); return x; }
DI float row16_sum(float x) { x += dpp<0xB1>(x); x += dpp<0x4E>(x); x += dpp<0x141>(x); x += dpp<0x140>(x); return x; }
DI void swap16(float x, float& a, float& b) { const int u = __builtin_bit_cast(int, x); auto r = __builtin_amdgcn_permlane16_swap(u, u, false, false); const int r0 = r[0], r1 = r[1]; a = __builtin_bit_cast(float, r0); b = __builtin_bit_cast(float, r1); }
DI void swap32(float x, float& a, float& b) { const int u = __builtin_bit_cast(int, x); auto r = __builtin_amdgcn_permlane32_swap(u, u, false, false); const int r0 = r[0], r1 = r[1]; a = __builtin_bit_cast(float, r0); b = __builtin_bit_cast(float, r1); }
DI float wave_max(float x) { float a, b; x = row16_max(x); swap16(x, a, b); x = fmaxf(a, b); swap32(x, a, b); return fmaxf(a, b); }
DI float wave_sum(float x) { float a, b; x = row16_sum(x); swap16(x, a, b); x = a + b; swap32(x, a, b); return a + b; }
DI float exp2f_(float x) { return __builtin_amdgcn_exp2f(x); }
DI float rcpf_(float x) { return __builtin_amdgcn_rcpf(x); }
DI rsrc_t mkrs(const void* p, u32 bytes) { return __builtin_amdgcn_make_buffer_rsrc((void*)p, 0, (int)bytes, 0x00020000); }
DI void dma16(u32 lds_addr, u32 voff, rsrc_t rs, u32 soff) {
  asm volatile("s_mov_b32 m0, %0\n\ts_nop 0\n\tbuffer_load_dwordx4 %1, %2, %3 offen lds" :: "s"(lds_addr), "v"(voff), "s"(rs), "s"(soff) : "memory");
}
#define WAITVM(n) asm volatile("s_waitcnt vmcnt(" #n ")" ::: "memory")
DI void bar() { asm volatile("s_waitcnt lgkmcnt(0)" ::: "memory"); __builtin_amdgcn_s_barrier(); asm volatile("" ::: "memory"); }
constexpr int AUX_SC1 = 16;
DI u32x4 ld16_sc1(rsrc_t rs, u32 off) { return __builtin_amdgcn_raw_buffer_load_b128(rs, off, 0, AUX_SC1); }
DI u32x2 ld8_sc1(rsrc_t rs, u32 off) { return __builtin_amdgcn_raw_buffer_load_b64(rs, off, 0, AUX_SC1); }
DI u32 ld4_sc1(rsrc_t rs, u32 off) { return __builtin_amdgcn_raw_buffer_load_b32(rs, off, 0, AUX_SC1); }
DI void st16_sc1(rsrc_t rs, u32 off, u32x4 v) { __builtin_amdgcn_raw_buffer_store_b128(v, rs, off, 0, AUX_SC1); }
DI void st8_sc1(rsrc_t rs, u32 off, u32x2 v) { __builtin_amdgcn_raw_buffer_store_b64(v, rs, off, 0, AUX_SC1); }
DI void st4_sc1(rsrc_t rs, u32 off, u32 v) { __builtin_amdgcn_raw_buffer_store_b32(v, rs, off, 0, AUX_SC1); }
constexpr u32 SPIN_CAP = 1u << 14;
DI bool spin_fail(u32& spins, rsrc_t rsws) {
  __builtin_amdgcn_s_sleep(2);
  asm volatile("" ::: "memory");
  if (++spins > SPIN_CAP) { st4_sc1(rsws, WS_TMO, 1u); return true; }
  return false;
}

DI u32 get_tag(u32x2 ev, rsrc_t rsws) {
  const u32 tag = (u32)__builtin_amdgcn_readfirstlane(ev.x) + 1u;
  asm volatile("" :: "v"(ev.y));
  if (threadIdx.x == 0) st4_sc1(rsws, WS_SEEN + (u32)blockIdx.x * 4, tag);
  return tag;
}
struct KIdx { u32 v[2080]; };
constexpr int lb_round(int l) {
  if (l < 256) return l;
  int sh = 0; while ((l >> sh) >= 256) ++sh;
  int step = 1 << sh, base = (l >> sh) << sh, rem = l - base, half = step >> 1;
  if (rem > half || (rem == half && ((l >> sh) & 1))) base += step;
  return base;
}
constexpr int kcompact(int v) {
  if (v < 256) { int i = (v & 7) + 8 * (v >> 7), j = (v >> 3) & 15; return i * 16 + j; }
  if (v < 512) { int o = v - 256; int i = 16 + ((o >> 1) & 3) + 4 * (o >> 7), j = (o >> 3) & 15; return i * 16 + j; }
  if (v < 1024) { int o = v - 512; int i = 24 + ((o >> 2) & 1) + 2 * (o >> 7), j = (o >> 3) & 15; return i * 16 + j; }
  if (v < 2048) { int o = v - 1024; int i = 32 + (o >> 7), j = (o >> 3) & 15; return i * 16 + j; }
  return 40 * 16;
}
constexpr int KCOPY_CH = 33040;
constexpr int LB_U = 2 * KCOPY_CH;
constexpr int LB_KC = LB_U + 2 * 4608;
constexpr int LB_MISC = LB_KC + 2 * 1536;
constexpr int LB_STG = 0;
constexpr int LB_KT = 4 * 8448;
constexpr int LB_TT = LB_KT + 12288;
constexpr int LDS_BYTES = 78848;
constexpr KIdx make_kidx() {
  KIdx t{};
  for (int q = 0; q < 2080; ++q) { int l = 2048 - q; t.v[q] = (u32)(LB_KC + ((l >= 0 && l < 2048) ? 2 * kcompact(lb_round(l)) : 2 * 767)); }
  return t;
}
__device__ const KIdx g_kidx = make_kidx();

constexpr int LA_WK = 0, LA_WV = 28672, LA_XS = 57344, LA_WP = 73728;
constexpr int LA_WQ = LA_WK, LA_RED = LA_WV;

DI void softmax32(const f32x16& pin, bool hi, u32x4 (&fp)[2], u32x4 (&fn)[2]) {
  float t[16];
#pragma unroll
  for (int i = 0; i < 8; ++i) { const u32 k = pk_bf16(pin[2 * i], pin[2 * i + 1]); t[2 * i] = bf2f(k) * LOG2E; t[2 * i + 1] = bfhi(k) * LOG2E; }
  float tp[4], tn[4];
#pragma unroll
  for (int i = 0; i < 4; ++i) { tp[i] = hi ? -__builtin_inff() : t[12 + i]; tn[i] = hi ? __builtin_inff() : t[12 + i]; }
  float mx = fmaxf(fmaxf(t[0], t[1]), t[2]), mn = fminf(fminf(t[0], t[1]), t[2]);
#pragma unroll
  for (int i = 3; i < 11; i += 2) { mx = fmaxf(fmaxf(mx, t[i]), t[i + 1]); mn = fminf(fminf(mn, t[i]), t[i + 1]); }
  mx = fmaxf(fmaxf(mx, t[11]), tp[0]); mx = fmaxf(fmaxf(mx, tp[1]), tp[2]); mx = fmaxf(mx, tp[3]);
  mn = fminf(fminf(mn, t[11]), tn[0]); mn = fminf(fminf(mn, tn[1]), tn[2]); mn = fminf(mn, tn[3]);
  { float a, b; swap32(mx, a, b); mx = fmaxf(a, b); swap32(mn, a, b); mn = fminf(a, b); }
  float ep[16], en[16];
#pragma unroll
  for (int i = 0; i < 12; ++i) { ep[i] = exp2f_(t[i] - mx); en[i] = exp2f_(mn - t[i]); }
#pragma unroll
  for (int i = 0; i < 4; ++i) { ep[12 + i] = exp2f_(tp[i] - mx); en[12 + i] = exp2f_(mn - tn[i]); }
  f32x2 sp2 = {ep[0], ep[1]}, sn2 = {en[0], en[1]};
#pragma unroll
  for (int i = 2; i < 16; i += 2) { sp2 += (f32x2){ep[i], ep[i + 1]}; sn2 += (f32x2){en[i], en[i + 1]}; }
  float sp = sp2[0] + sp2[1], sn = sn2[0] + sn2[1];
  { float a, b; swap32(sp, a, b); sp = a + b; swap32(sn, a, b); sn = a + b; }
  const float ip = rcpf_(sp), in = rcpf_(sn);
#pragma unroll
  for (int s = 0; s < 2; ++s)
#pragma unroll
    for (int k = 0; k < 4; ++k) {
      fp[s][k] = pk_bf16(ep[8 * s + 2 * k] * ip, ep[8 * s + 2 * k + 1] * ip);
      fn[s][k] = pk_bf16(en[8 * s + 2 * k] * in, en[8 * s + 2 * k + 1] * in);
    }
}
DI u32x4 pack8(const f32x16& a, int s) { return (u32x4){pk_bf16(a[8 * s], a[8 * s + 1]), pk_bf16(a[8 * s + 2], a[8 * s + 3]), pk_bf16(a[8 * s + 4], a[8 * s + 5]), pk_bf16(a[8 * s + 6], a[8 * s + 7])}; }

DI void roleA(const Params& p, int h, int rr, LAS char* lds, u32x2 ev, rsrc_t rsws) {
  const int tid = threadIdx.x, lane = tid & 63, w = __builtin_amdgcn_readfirstlane(tid >> 6);
  const int l32 = lane & 31, hh = lane >> 5;
  const bool hi = lane >= 32;
  __builtin_amdgcn_s_setprio(2);
  const u32 ldsb = (u32)(uintptr_t)lds;
  const rsrc_t rsW = mkrs(p.wqkv, D * 3 * D * 2), rsP = mkrs(p.wphi, H * DH * DH * 2);
  const u32 wvoff = ((lane >> 2) * (3 * D) + h * DH) * 2 + (lane & 3) * 16;
  const int r0 = rr * 128 + w * 32;
  dma16(ldsb + LA_WP + (w & 1) * 1024, (h * (DH * DH) + ((w & 1) * 16 + (lane >> 2)) * DH) * 2 + (lane & 3) * 16, rsP, 0);
  u32x4 xr[7][4];
  {
    const u16* xb = p.x + (size_t)(r0 + (lane >> 3)) * D + 8 * (lane & 7);
    const u16* xb1 = xb + 8 * D; const u16* xb2 = xb + 16 * D; const u16* xb3 = xb + 24 * D;
    const u32 wv2 = wvoff + (1 + (w >> 1)) * (D * 2);
    const u32 lw = ldsb + LA_WK + (w >> 1) * 28672 + (w & 1) * 2048;
    const u32 so = (w & 1) * 32 * (3 * D * 2);
#pragma unroll
    for (int c = 0; c < 7; ++c) {
      dma16(lw + c * 4096, wv2, rsW, so + c * 64 * (3 * D * 2));
      dma16(lw + c * 4096 + 1024, wv2, rsW, so + (c * 64 + 16) * (3 * D * 2));
      asm volatile("global_load_dwordx4 %0, %1, off offset:%2" : "=v"(xr[c][0]) : "v"(xb), "i"(128 * c) : "memory");
      asm volatile("global_load_dwordx4 %0, %1, off offset:%2" : "=v"(xr[c][1]) : "v"(xb1), "i"(128 * c) : "memory");
      asm volatile("global_load_dwordx4 %0, %1, off offset:%2" : "=v"(xr[c][2]) : "v"(xb2), "i"(128 * c) : "memory");
      asm volatile("global_load_dwordx4 %0, %1, off offset:%2" : "=v"(xr[c][3]) : "v"(xb3), "i"(128 * c) : "memory");
    }
  }
  LAS char* xs = lds + LA_XS + w * 4096;
  const int r3 = lane >> 3, ch8 = lane & 7;
  LAS char* wa0 = xs + r3 * 128 + ((ch8 ^ (r3 >> 1)) << 4);
  LAS char* wa1 = xs + r3 * 128 + ((ch8 ^ (r3 >> 1) ^ 4) << 4);
  LAS char* ra[4];
#pragma unroll
  for (int j = 0; j < 4; ++j) ra[j] = xs + l32 * 128 + (((2 * j + hh) ^ ((l32 >> 1) & 7)) << 4);
  LAS char* wb = lds + (8 * hh + ((lane & 15) >> 2)) * 64 + 32 * ((lane >> 4) & 1) + 8 * (lane & 3);
  u32x4 xf[28];
  f32x16 ak, av;
#pragma unroll
  for (int i = 0; i < 16; ++i) { ak[i] = 0.f; av[i] = 0.f; }
  u32x4 wk[2][4], wv[2][4];
  u32 tag = 0;
#define WAITX(c, n) asm volatile("s_waitcnt vmcnt(" #n ")" : "+v"(xr[c][0]), "+v"(xr[c][1]), "+v"(xr[c][2]), "+v"(xr[c][3]) :: "memory")
#pragma unroll
  for (int c = 0; c < 8; ++c) {
    if (c < 7) {
      if (c == 0) WAITX(0, 36); else if (c == 1) WAITX(1, 30); else if (c == 2) WAITX(2, 24); else if (c == 3) WAITX(3, 18);
      else if (c == 4) WAITX(4, 12); else if (c == 5) WAITX(5, 6); else WAITX(6, 0);
      __builtin_amdgcn_s_barrier();
      asm volatile("" ::: "memory");
      if (c == 0) STAMP(0, 1);
      *(LAS u32x4*)(wa0) = xr[c][0]; *(LAS u32x4*)(wa1 + 1024) = xr[c][1]; *(LAS u32x4*)(wa0 + 2048) = xr[c][2]; *(LAS u32x4*)(wa1 + 3072) = xr[c][3];
#pragma unroll
      for (int j = 0; j < 4; ++j) {
        const int ks = 4 * c + j;
        xf[ks] = *(LAS u32x4*)(ra[j]);
        wk[c & 1][j] = cat(tr_read(wb + LA_WK + ks * 1024), tr_read(wb + LA_WK + ks * 1024 + 256));
        wv[c & 1][j] = cat(tr_read(wb + LA_WV + ks * 1024), tr_read(wb + LA_WV + ks * 1024 + 256));
      }
    }
    if (c > 0) {
#pragma unroll
      for (int j = 0; j < 4; ++j) {
        const int ks = 4 * (c - 1) + j;
        ak = mfma32(wk[(c - 1) & 1][j], xf[ks], ak);
        av = mfma32(xf[ks], wv[(c - 1) & 1][j], av);
      }
    }
    if (c < 7) {
#pragma unroll
      for (int j = 0; j < 8; ++j) { __builtin_amdgcn_sched_group_barrier(0x300, 3, 0); if (c > 0) __builtin_amdgcn_sched_group_barrier(0x8, 1, 0); }
    }
    __builtin_amdgcn_sched_barrier(0);
    if (c == 6) tag = get_tag(ev, rsws);
  }
  STAMP(0, 2);
  u32x4 wf[2];
  {
    LAS char* pb = lds + LA_WP + (4 * hh + ((lane & 15) >> 2)) * 64 + 32 * ((lane >> 4) & 1) + 8 * (lane & 3);
#pragma unroll
    for (int s = 0; s < 2; ++s) wf[s] = cat(tr_read(pb + s * 1024), tr_read(pb + s * 1024 + 512));
  }
  bar();
#pragma unroll
  for (int i = 0; i < 7; ++i) dma16(ldsb + LA_WQ + (w * 7 + i) * 1024, wvoff, rsW, (w * 7 + i) * 16 * (3 * D * 2));
  f32x16 z16;
#pragma unroll
  for (int i = 0; i < 16; ++i) z16[i] = 0.f;
  {
    u32x4 k0 = pack8(ak, 0), k1 = pack8(ak, 1);
    if (hi) { k1[2] = 0; k1[3] = 0; }
    f32x16 pk = mfma32(wf[0], k0, z16);
    pk = mfma32(wf[1], k1, pk);
    u32x4 fp[2], fn[2];
    softmax32(pk, hi, fp, fn);
    const int sw = (l32 >> 1) & 7;
#pragma unroll
    for (int s = 0; s < 2; ++s) {
      *(LAS u32x2*)(xs + l32 * 64 + 8 * ((4 * s + hh) ^ sw)) = (u32x2){fp[s][0], fp[s][1]};
      *(LAS u32x2*)(xs + l32 * 64 + 8 * ((4 * s + 2 + hh) ^ sw)) = (u32x2){fp[s][2], fp[s][3]};
      *(LAS u32x2*)(xs + 2048 + l32 * 64 + 8 * ((4 * s + hh) ^ sw)) = (u32x2){fn[s][0], fn[s][1]};
      *(LAS u32x2*)(xs + 2048 + l32 * 64 + 8 * ((4 * s + 2 + hh) ^ sw)) = (u32x2){fn[s][2], fn[s][3]};
    }
    const int q = (lane & 15) >> 2, pp = lane & 3, cb = (lane >> 4) & 1;
    const int u = 4 * cb + pp, sx = 2 * hh + (q >> 1);
    LAS char* t0 = xs + (4 * hh + q) * 64 + 8 * (u ^ sx);
    LAS char* t1 = xs + (4 * hh + q + 8) * 64 + 8 * (u ^ sx ^ 4);
    u32x4 vb[2] = {pack8(av, 0), pack8(av, 1)};
    if (l32 == 28) { vb[0] = (u32x4){0x3f803f80u, 0x3f803f80u, 0x3f803f80u, 0x3f803f80u}; vb[1] = vb[0]; }
    f32x16 kva[2];
#pragma unroll
    for (int sg = 0; sg < 2; ++sg) {
      const u32x4 a0 = cat(tr_read(t0 + sg * 2048), tr_read(t1 + sg * 2048));
      const u32x4 a1 = cat(tr_read(t0 + sg * 2048 + 1024), tr_read(t1 + sg * 2048 + 1024));
      kva[sg] = mfma32(a0, vb[0], z16);
      kva[sg] = mfma32(a1, vb[1], kva[sg]);
    }
#pragma unroll
    for (int gidx = 0; gidx < 4; ++gidx) {
      LAS char* dst = (gidx == w) ? xs : lds + LA_RED + (gidx * 3 + (w > gidx ? w - 1 : w)) * 2048;
      const f32x16& a = kva[gidx >> 1];
      const int o = 8 * (gidx & 1);
      *(LAS f32x4*)(dst + lane * 16) = (f32x4){a[o], a[o + 1], a[o + 2], a[o + 3]};
      *(LAS f32x4*)(dst + 1024 + lane * 16) = (f32x4){a[o + 4], a[o + 5], a[o + 6], a[o + 7]};
    }
  }
  WAITVM(0);
  bar();
  {
    f32x4 s0 = {0, 0, 0, 0}, s1 = {0, 0, 0, 0};
#pragma unroll
    for (int src = 0; src < 4; ++src) {
      LAS char* b = (src == w) ? xs : lds + LA_RED + (w * 3 + (src > w ? src - 1 : src)) * 2048;
      s0 += *(LAS f32x4*)(b + lane * 16); s1 += *(LAS f32x4*)(b + 1024 + lane * 16);
    }
    const u32 base = WS_G1 + (u32)((h * 16 + rr) * 16 + 4 * w) * 1024 + lane * 16;
    st16_sc1(rsws, base, (u32x4){f2u(s0[0]), tag, f2u(s0[1]), tag});
    st16_sc1(rsws, base + 1024, (u32x4){f2u(s0[2]), tag, f2u(s0[3]), tag});
    st16_sc1(rsws, base + 2048, (u32x4){f2u(s1[0]), tag, f2u(s1[1]), tag});
    st16_sc1(rsws, base + 3072, (u32x4){f2u(s1[2]), tag, f2u(s1[3]), tag});
  }
  STAMP(0, 3);
  if (w == 0) {
    const u32 base = WS_G1 + (u32)(h * 16 * 16 + rr) * 1024 + lane * 16;
    u32 spins = 0;
    float a = 0.f, b = 0.f;
    for (;;) {
      u32x4 v[16];
#pragma unroll
      for (int s = 0; s < 16; ++s) v[s] = ld16_sc1(rsws, base + s * 16384);
      bool ok = true; a = 0.f; b = 0.f;
#pragma unroll
      for (int s = 0; s < 16; ++s) { ok &= (v[s][1] == tag) & (v[s][3] == tag); const u32 x0 = v[s][0], x1 = v[s][2]; a += __builtin_bit_cast(float, x0); b += __builtin_bit_cast(float, x1); }
      if (__all(ok) || spin_fail(spins, rsws)) break;
    }
    st8_sc1(rsws, WS_G2 + (u32)(h * 16 + rr) * 512 + lane * 8, (u32x2){pk_bf16(a, b), tag});
    STAMP(0, 4);
  }
  f32x16 aq = z16;
#pragma unroll
  for (int ks = 0; ks < 28; ++ks) {
    const u32x4 wq = cat(tr_read(wb + LA_WQ + ks * 1024), tr_read(wb + LA_WQ + ks * 1024 + 256));
    aq = mfma32(wq, xf[ks], aq);
  }
  u32x4 fp[2], fn[2];
  {
    u32x4 q0 = pack8(aq, 0), q1 = pack8(aq, 1);
    if (hi) { q1[2] = 0; q1[3] = 0; }
    f32x16 pq = mfma32(wf[0], q0, z16);
    pq = mfma32(wf[1], q1, pq);
    softmax32(pq, hi, fp, fn);
  }
  STAMP(0, 5);
  LAS char* kvl = lds + LA_RED;
  kvl = lds + LA_XS;
  if (w == 0) {
    const u32 base = WS_G2 + (u32)(h * 16) * 512 + lane * 8;
    u32 spins = 0;
    u32x2 v[16];
    for (;;) {
#pragma unroll
      for (int s = 0; s < 16; ++s) v[s] = ld8_sc1(rsws, base + s * 512);
      bool ok = true;
#pragma unroll
      for (int s = 0; s < 16; ++s) ok &= v[s][1] == tag;
      if (__all(ok) || spin_fail(spins, rsws)) break;
    }
#pragma unroll
    for (int f = 0; f < 4; ++f) *(LAS u32x4*)(kvl + f * 1024 + lane * 16) = (u32x4){v[4 * f][0], v[4 * f + 1][0], v[4 * f + 2][0], v[4 * f + 3][0]};
  }
  bar();
  STAMP(0, 6);
  {
    f32x16 n = mfma32(*(LAS u32x4*)(kvl + lane * 16), fp[0], z16);
    n = mfma32(*(LAS u32x4*)(kvl + 1024 + lane * 16), fp[1], n);
    n = mfma32(*(LAS u32x4*)(kvl + 2048 + lane * 16), fn[0], n);
    n = mfma32(*(LAS u32x4*)(kvl + 3072 + lane * 16), fn[1], n);
    float a, b; swap32(n[12], a, b);
    const float id = rcpf_(rbf(rbf(b) + 1e-6f));
    u32x4 of[2];
#pragma unroll
    for (int s = 0; s < 2; ++s)
#pragma unroll
      for (int k = 0; k < 4; ++k) {
        const u32 r = pk_bf16(n[8 * s + 2 * k], n[8 * s + 2 * k + 1]);
        of[s][k] = pk_bf16(bf2f(r) * id, bfhi(r) * id);
      }
    if (hi) { of[1][2] = 0; of[1][3] = 0; }
    u32x4* dst = (u32x4*)(p.ws + WS_OF) + (size_t)(((r0 >> 5) * 16 + h) * 2) * 64 + lane;
    dst[0] = of[0]; dst[64] = of[1];
  }
  STAMP(0, 7);
}

__device__ constexpr int KBASE[8] = {258, 1807, 1290, 0, 774, 516, 1032, 1548};
struct cpx2 { f32x2 re, im; };
DI cpx2 cmul(const cpx2& a, const cpx2& b) { cpx2 r; r.re = a.re * b.re - a.im * b.im; r.im = a.re * b.im + a.im * b.re; return r; }
DI cpx2 csqr(const cpx2& a) { cpx2 r; r.re = a.re * a.re - a.im * a.im; r.im = (a.re + a.re) * a.im; return r; }
DI cpx2 cexp_m(f32x2 al, f32x2 th, float m) {
  cpx2 r;
#pragma unroll
  for (int i = 0; i < 2; ++i) {
    const float mag = exp2f_(al[i] * m);
    const float ph = __builtin_amdgcn_fractf(th[i] * m);
    r.re[i] = mag * __builtin_amdgcn_cosf(ph); r.im[i] = mag * __builtin_amdgcn_sinf(ph);
  }
  return r;
}
DI void put_row(LAS char* stg, int i, const cpx2& v) { *(LAS u32x2*)(stg + i * 528) = (u32x2){pk_f16(v.re[0], v.im[0]), pk_f16(v.re[1], v.im[1])}; }
DI void put_rowc(LAS char* stg, int i, const cpx2& v) { *(LAS u32x2*)(stg + i * 528) = (u32x2){pk_f16(v.re[0], -v.im[0]), pk_f16(v.re[1], -v.im[1])}; }

template <int ROLE> DI void conv_role(LAS char* abase, LAS char* bbase, LAS char* ubase, u16* ydst, float inv, float isu, float dsk, int lane) {
  constexpr int NCB = ROLE == 0 ? 3 : 5;
  constexpr int CB[5] = {ROLE == 0 ? 7 : 5, ROLE == 0 ? 6 : 4, ROLE == 0 ? 2 : 3, 1, 0};
  constexpr int MAXI = ROLE == 0 ? 8 : 6;
  f32x4 acc[NCB];
#pragma unroll
  for (int i = 0; i < NCB; ++i) acc[i] = (f32x4){0, 0, 0, 0};
#pragma unroll 1
  for (int it = 0; it < 8; ++it) {
    LAS char* a = abase - 64 * it;
    LAS char* b = bbase - 64 * it;
    u32x4 A[MAXI], B[MAXI];
#pragma unroll
    for (int s = 0; s < MAXI; ++s) { A[s] = *(LAS u32x4*)(a + 512 * (7 - s)); B[s] = *(LAS u32x4*)(b + 512 * s); }
#pragma unroll
    for (int i = 0; i < NCB; ++i)
#pragma unroll
      for (int s = 0; s <= CB[i]; ++s) acc[i] = mfma_h(A[s], B[CB[i] - s], acc[i]);
  }
  const int l16 = lane & 15, g = lane >> 4;
#pragma unroll
  for (int i = 0; i < NCB; ++i) {
    const int l0 = 256 * CB[i] + 16 * l16 + 4 * g;
    const f16x4 uh = *(LAS f16x4*)(ubase + l0 * 2);
    float o[4];
#pragma unroll
    for (int r = 0; r < 4; ++r) o[r] = rbf(acc[i][r] * inv) + rbf((float)uh[r] * isu * dsk);
    *(u32x2*)(ydst + l0) = (u32x2){pk_bf16(o[0], o[1]), pk_bf16(o[2], o[3])};
  }
}
DI float pow2_scale(float mx, int target) {
  const int e = (int)((__builtin_bit_cast(u32, mx) >> 23) & 0xff);
  int se = 254 + target - e;
  se = se < 1 ? 1 : (se > 254 ? 254 : se);
  return __builtin_bit_cast(float, (u32)se << 23);
}

DI void roleB(const Params& p, int bb, LAS char* lds, u32x2 ev, rsrc_t rsws) {
  const int tid = threadIdx.x, lane = tid & 63, w = __builtin_amdgcn_readfirstlane(tid >> 6);
  const int l16 = lane & 15, g = lane >> 4;
  const int pair = (bb & 7) * 28 + (bb >> 3);
  const int d0 = 2 * pair;
  const int ch = w >> 1, half = w & 1, d = d0 + ch;
  LAS float* misc = (LAS float*)(lds + LB_MISC);
  const int trt = bb / 7, tcg = bb - 7 * trt;
  u32x4 tx[2];
#pragma unroll
  for (int i = 0; i < 2; ++i) tx[i] = *(const u32x4*)(p.x + (size_t)(64 * trt + 32 * i + (tid >> 3)) * D + 64 * tcg + 8 * (tid & 7));
  const int pi = (d * NS + half * 128 + 2 * lane) >> 1;
  const u32 are = ((const u32*)p.Are)[pi], aim = ((const u32*)p.Aim)[pi], bre = ((const u32*)p.Bre)[pi], bim = ((const u32*)p.Bim)[pi], cre = ((const u32*)p.Cre)[pi], cim = ((const u32*)p.Cim)[pi];
  const float ldt = bf2f(p.logdt[d]);
  const float dsk = bf2f(p.dskip[d]);
  {
    LAS char* tt = lds + LB_TT;
#pragma unroll
    for (int i = 0; i < 2; ++i) { const int row = 32 * i + (tid >> 3); *(LAS u32x4*)(tt + row * 128 + (((tid & 7) ^ (row & 7)) << 4)) = tx[i]; }
    bar();
    const int q = l16 >> 2, pp = lane & 3;
#pragma unroll
    for (int i = 0; i < 2; ++i) {
      const int lc = g + 4 * i;
      const int ra = 8 * lc + q, rb = ra + 4;
      const int cu = 2 * (2 * w) + (pp >> 1);
      (void)cu;
      const int chunk = 2 * w + (pp >> 1), sub = (pp & 1) * 8;
      const u32x2 lo = tr_read(tt + ra * 128 + ((chunk ^ (ra & 7)) << 4) + sub);
      const u32x2 hi = tr_read(tt + rb * 128 + ((chunk ^ (rb & 7)) << 4) + sub);
      st16_sc1(rsws, WS_XT + (u32)((64 * tcg + 16 * w + l16) * S + 64 * trt + 8 * lc) * 2, cat(lo, hi));
    }
  }
  const u32 tag = get_tag(ev, rsws);
  STAMP(0, 1);
  const float dt = rbf(__expf(ldt));
  const f32x2 al = (f32x2){bf2f(are), bfhi(are)} * (dt * LOG2E);
  const f32x2 th = (f32x2){bf2f(aim), bfhi(aim)} * dt * INV2PI;
  const f32x2 br = (f32x2){bf2f(bre), bfhi(bre)} * dt, bi = (f32x2){bf2f(bim), bfhi(bim)} * dt;
  const f32x2 cr = {bf2f(cre), bfhi(cre)}, ci = {bf2f(cim), bfhi(cim)};
  cpx2 W; W.re = cr * br - ci * bi; W.im = cr * bi + ci * br;
  const float wm0 = __builtin_amdgcn_sqrtf(W.re[0] * W.re[0] + W.im[0] * W.im[0]) * exp2f_(fmaxf(0.f, al[0] * 2048.f));
  const float wm1 = __builtin_amdgcn_sqrtf(W.re[1] * W.re[1] + W.im[1] * W.im[1]) * exp2f_(fmaxf(0.f, al[1] * 2048.f));
  const float wmax = wave_max(fmaxf(wm0, wm1)), wsum = wave_sum(wm0 + wm1);
  const float SR = pow2_scale(wmax, 14);
  W.re *= SR; W.im *= SR;
  LAS char* stgr = lds + LB_STG + w * 8448;
  LAS char* stg = stgr + lane * 8;
  LAS char* frd = stgr + l16 * 528 + 16 * g;
  const cpx2 Z8 = cexp_m(al, th, 8.f);
  const cpx2 Z16 = csqr(Z8), Z32 = csqr(Z16), Z64 = csqr(Z32);
  {
    cpx2 C[8];
    C[0].re = (f32x2){1.f, 1.f}; C[0].im = (f32x2){0.f, 0.f};
    C[1] = Z8; C[2] = Z16; C[3] = cmul(Z8, Z16); C[4] = Z32; C[5] = cmul(Z32, Z8); C[6] = cmul(Z32, Z16); C[7] = cmul(Z32, C[3]);
#pragma unroll
    for (int j = 0; j < 8; ++j) put_rowc(stg, j, C[j]);
    put_rowc(stg, 8, Z64);
#pragma unroll
    for (int j = 1; j < 8; ++j) put_rowc(stg, 8 + j, cmul(Z64, C[j]));
  }
  u32x4 Bf[8];
#pragma unroll
  for (int ks = 0; ks < 8; ++ks) Bf[ks] = *(LAS u32x4*)(frd + 64 * ks);
  WAITVM(0);
  bar();
  if (tid == 0) st4_sc1(rsws, WS_XFLAG + (u32)(tcg * 32 + trt) * 4, tag);
  STAMP(0, 2);
  const cpx2 Z1 = cexp_m(al, th, 1.f);
  const cpx2 Z2 = csqr(Z1), Z4 = csqr(Z2);
  cpx2 Sm[8];
  Sm[1] = Z1; Sm[2] = Z2; Sm[3] = cmul(Z1, Z2); Sm[4] = Z4; Sm[5] = cmul(Z4, Z1); Sm[6] = cmul(Z4, Z2); Sm[7] = cmul(Z4, Sm[3]);
  const cpx2 Z128 = cexp_m(al, th, 128.f);
  const cpx2 Z256 = csqr(Z128), Z512 = csqr(Z256);
  const cpx2 Z1024 = cexp_m(al, th, 1024.f);
  const cpx2 Z2048 = csqr(Z1024);
  f32x4 kacc[3];
  {
    const cpx2 B1 = cmul(W, Z128);
    put_row(stg, 0, W);
#pragma unroll
    for (int i = 1; i < 8; ++i) put_row(stg, i, cmul(W, Sm[i]));
    put_row(stg, 8, B1);
#pragma unroll
    for (int i = 1; i < 8; ++i) put_row(stg, 8 + i, cmul(B1, Sm[i]));
    f32x4 acc = {0, 0, 0, 0};
#pragma unroll
    for (int ks = 0; ks < 8; ++ks) acc = mfma_h(*(LAS u32x4*)(frd + 64 * ks), Bf[ks], acc);
    kacc[0] = acc;
  }
  const cpx2 Z384 = cmul(Z256, Z128);
  {
    const cpx2 B2 = cmul(W, Z256), B3 = cmul(W, Z384), B4 = cmul(W, Z512);
    const cpx2 B5 = cmul(B4, Z128), B6 = cmul(B4, Z256), B7 = cmul(B4, Z384);
    put_row(stg, 0, B2); put_row(stg, 1, cmul(B2, Sm[2])); put_row(stg, 2, cmul(B2, Sm[4])); put_row(stg, 3, cmul(B2, Sm[6]));
    put_row(stg, 4, B3); put_row(stg, 5, cmul(B3, Sm[2])); put_row(stg, 6, cmul(B3, Sm[4])); put_row(stg, 7, cmul(B3, Sm[6]));
    put_row(stg, 8, B4); put_row(stg, 9, cmul(B4, Sm[4])); put_row(stg, 10, B5); put_row(stg, 11, cmul(B5, Sm[4]));
    put_row(stg, 12, B6); put_row(stg, 13, cmul(B6, Sm[4])); put_row(stg, 14, B7); put_row(stg, 15, cmul(B7, Sm[4]));
    f32x4 acc = {0, 0, 0, 0};
#pragma unroll
    for (int ks = 0; ks < 8; ++ks) acc = mfma_h(*(LAS u32x4*)(frd + 64 * ks), Bf[ks], acc);
    kacc[1] = acc;
  }
  {
    const cpx2 B8 = cmul(W, Z1024);
    const cpx2 Z640 = cmul(Z512, Z128), Z768 = cmul(Z512, Z256), Z896 = cmul(Z512, Z384);
    put_row(stg, 0, B8); put_row(stg, 1, cmul(B8, Z128)); put_row(stg, 2, cmul(B8, Z256)); put_row(stg, 3, cmul(B8, Z384));
    put_row(stg, 4, cmul(B8, Z512)); put_row(stg, 5, cmul(B8, Z640)); put_row(stg, 6, cmul(B8, Z768)); put_row(stg, 7, cmul(B8, Z896));
    put_row(stg, 8, cmul(W, Z2048));
    f32x4 acc = {0, 0, 0, 0};
#pragma unroll
    for (int ks = 0; ks < 8; ++ks) acc = mfma_h(*(LAS u32x4*)(frd + 64 * ks), Bf[ks], acc);
    kacc[2] = acc;
  }
  {
    LAS float* kt = (LAS float*)(lds + LB_KT) + (ch * 2 + half) * 768;
    const float isr = rcpf_(SR);
#pragma unroll
    for (int t = 0; t < 3; ++t)
#pragma unroll
      for (int r = 0; r < 4; ++r) kt[(16 * t + 4 * g + r) * 16 + l16] = kacc[t][r] * isr;
    if (lane == 0) misc[w] = wsum;
  }
  u32x4 kix[4];
#pragma unroll
  for (int i = 0; i < 4; ++i) kix[i] = *(const u32x4*)(g_kidx.v + 8 * tid + 4 * i);
  STAMP(0, 3);
  u32x4 uv[4];
  {
    const u32 fo = WS_XFLAG + (u32)((d0 >> 6) * 32 + (lane & 31)) * 4;
    u32 spins = 0;
    for (;;) { const u32 f = ld4_sc1(rsws, fo); if (__all(f == tag) || spin_fail(spins, rsws)) break; }
#pragma unroll
    for (int i = 0; i < 4; ++i) uv[i] = ld16_sc1(rsws, WS_XT + (u32)(d * S) * 2 + (u32)(lane + 64 * (i ^ (2 * half))) * 16);
  }
  bar();
  STAMP(0, 4);
  const float SK0 = pow2_scale(misc[0] + misc[1], 14), SK1 = pow2_scale(misc[2] + misc[3], 14);
  {
    LAS float* kt = (LAS float*)(lds + LB_KT);
#pragma unroll
    for (int i = 0; i < 6; ++i) {
      const int e = tid + 256 * i, c = i >= 3, idx = e - 768 * c;
      float v = (kt[(c * 2) * 768 + idx] + kt[(c * 2 + 1) * 768 + idx]) * (c ? SK1 : SK0);
      if (idx >= 41 * 16) v = 0.f;
      *(LAS _Float16*)(lds + LB_KC + c * 1536 + idx * 2) = (_Float16)v;
    }
  }
  bar();
#pragma unroll
  for (int c = 0; c < 2; ++c) {
    LAS char* cp = lds + c * KCOPY_CH;
#pragma unroll 1
    for (int pass = 0; pass < 2; ++pass) {
      if (pass == 1 && tid >= 2) break;
      u32x4 ix[4]; int m = tid;
#pragma unroll
      for (int i = 0; i < 4; ++i) ix[i] = kix[i];
      if (pass == 1) {
        m = 256 + tid;
#pragma unroll
        for (int i = 0; i < 4; ++i) ix[i] = *(const u32x4*)(g_kidx.v + 8 * m + 4 * i);
      }
      u32 dw[8];
#pragma unroll
      for (int k = 0; k < 8; ++k) {
        u16x2 v;
        v.x = *(LAS u16*)(lds + c * 1536 + ix[k >> 1][(2 * k) & 3]);
        v.y = *(LAS u16*)(lds + c * 1536 + ix[k >> 1][(2 * k + 1) & 3]);
        dw[k] = __builtin_bit_cast(u32, v);
      }
      u32 od[7];
#pragma unroll
      for (int k = 0; k < 7; ++k) od[k] = __builtin_amdgcn_alignbit(dw[k + 1], dw[k], 16);
#pragma unroll
      for (int s = 0; s < 4; ++s) {
        *(LAS u32x4*)(cp + (KBASE[2 * s] + m) * 16) = (u32x4){dw[s], dw[s + 1], dw[s + 2], dw[s + 3]};
        *(LAS u32x4*)(cp + (KBASE[2 * s + 1] + m) * 16) = (u32x4){od[s], od[s + 1], od[s + 2], od[s + 3]};
      }
    }
  }
  float SU;
  {
    u32 m = 0;
#pragma unroll
    for (int i = 0; i < 4; ++i)
#pragma unroll
      for (int k = 0; k < 4; ++k) { const u32 a = uv[i][k] & 0x7fff7fffu; m = max(m, max(a << 16, a & 0xffff0000u)); }
    SU = pow2_scale(wave_max(__builtin_bit_cast(float, m) * 1.0f), 14);
    LAS char* ub = lds + LB_U + ch * 4608;
#pragma unroll
    for (int i = 0; i < 2; ++i) {
      const u32x4 v = uv[i];
      u32x4 o;
#pragma unroll
      for (int k = 0; k < 4; ++k) o[k] = pk_f16(bf2f(v[k]) * SU, bfhi(v[k]) * SU);
      *(LAS u32x4*)(ub + 512 + (lane + 64 * (2 * half + i)) * 16) = o;
    }
    if (half == 0 && lane < 32) *(LAS u32x4*)(ub + lane * 16) = (u32x4){0, 0, 0, 0};
  }
  bar();
  STAMP(0, 5);
  {
    const int cc = (8 - (l16 & 7)) & 7;
    int kb = 0;
#pragma unroll
    for (int i = 0; i < 8; ++i) if (cc == i) kb = KBASE[i];
    const int m113 = (2048 - 16 * 113 - l16 + 8 * g - cc) >> 3;
    LAS char* abase = lds + ch * KCOPY_CH + (kb + m113) * 16;
    LAS char* bbase = lds + LB_U + ch * 4608 + (256 + 16 * (l16 - 1) + 8 * g) * 2;
    const float inv = rcpf_((ch ? SK1 : SK0) * SU);
    u16* ydst = (u16*)(p.ws + WS_YT) + (size_t)d * S;
    LAS char* ubase = lds + LB_U + ch * 4608 + 512;
    const float isu = rcpf_(SU);
    if (half == 0) conv_role<0>(abase, bbase, ubase, ydst, inv, isu, dsk, lane); else conv_role<1>(abase, bbase, ubase, ydst, inv, isu, dsk, lane);
  }
  STAMP(0, 6);
}

DI void roleG(const Params& p, int gb, u32x2 ev, rsrc_t rsws) {
  const int tid = threadIdx.x;
  const u32 tag = get_tag(ev, rsws);
  const int row = gb * 64 + (tid >> 2), part = tid & 3;
  float s = 0.f;
#pragma unroll
  for (int i = 0; i < 14; ++i) {
    const u32x4 xv = *(const u32x4*)(p.x + (size_t)row * D + (i * 4 + part) * 8);
    const u32x4 gv = *(const u32x4*)(p.gatew + (i * 4 + part) * 8);
#pragma unroll
    for (int k = 0; k < 4; ++k) {
      const u32 r = pk_bf16(bf2f(xv[k]) * bf2f(gv[k]), bfhi(xv[k]) * bfhi(gv[k]));
      s += bf2f(r); s += bfhi(r);
    }
  }
  s += dpp<0xB1>(s); s += dpp<0x4E>(s);
  if (part == 0) { const float lg = rbf(s); ((float*)(p.ws + WS_G))[row] = rbf(rcpf_(1.f + exp2f_(-lg * LOG2E))); }
  if (gb == 0 && tid < 64) {
    u32 spins = 0;
    for (;;) {
      bool ok = true;
#pragma unroll
      for (int i = 0; i < 8; ++i) ok &= ld4_sc1(rsws, WS_SEEN + (u32)(tid + 64 * i) * 4) == tag;
      if (__all(ok) || spin_fail(spins, rsws)) break;
    }
    if (tid == 0) st4_sc1(rsws, WS_EPOCH, tag);
  }
}

__global__ void __launch_bounds__(256, 2) k_main(Params p) {
  extern __shared__ __attribute__((aligned(16))) char smem[];
  LAS char* lds = (LAS char*)smem;
  const rsrc_t rsws = mkrs(p.ws, WS_USED);
  const int b = blockIdx.x, xcd = b & 7, j = b >> 3;
  STAMP(0, 0);
  u32 pfv = 0;
  { u64 pc; asm volatile("s_getpc_b64 %0" : "=s"(pc)); const u32 off = threadIdx.x * 128;
    asm volatile("global_load_dword %0, %1, %2" : "=v"(pfv) : "v"(off), "s"(pc & ~127ull) : "memory"); }
  u32x2 ev = {ld4_sc1(rsws, WS_EPOCH), pfv};
  asm volatile("" ::: "memory");
  const bool isA = ((j + (j >> 5)) & 1) == 0;
  const int o = j >> 1;
  if (isA) { roleA(p, 8 * (xcd & 1) + (o & 7), 4 * (xcd >> 1) + (o >> 3), lds, ev, rsws); }
  else if (o < 28) { roleB(p, o * 8 + xcd, lds, ev, rsws); }
  else { roleG(p, (o - 28) * 8 + xcd, ev, rsws); }
}

constexpr int LF_B = 57 * 1024;
constexpr int LF_BYTES = LF_B + 8192;
__global__ void __launch_bounds__(256, 2) k_fin(Params p) {
  extern __shared__ __attribute__((aligned(16))) char smem[];
  LAS char* lds = (LAS char*)smem;
  const int tid = threadIdx.x, lane = tid & 63, w = __builtin_amdgcn_readfirstlane(tid >> 6);
  const int l16 = lane & 15, g = lane >> 4;
  const int b = blockIdx.x, xcd = b & 7, j = b >> 3;
  const int rg = 4 * xcd + j / 7, cg = j % 7;
  const int R0 = rg * 64 + 16 * w, C0 = 64 * cg;
  STAMP(1, 0);
  const u32 ldsb = (u32)(uintptr_t)lds;
  const int rsub = lane >> 3, pos = lane & 7;
  const int chs = (((pos >> 1) ^ ((rsub >> 1) & 3)) << 1) | (pos & 1);
  {
    const rsrc_t rs = mkrs(p.wo, D * D * 2);
    const u32 voff = (rsub * D + C0) * 2 + 16 * chs;
#pragma unroll
    for (int i = 0; i < 15; ++i) { const int blk = w + 4 * i; if (i < 14 || w == 0) dma16(ldsb + blk * 1024, voff, rs, blk * 8 * D * 2); }
    const rsrc_t rsy = mkrs(p.ws + WS_YT, D * S * 2);
    const u32 vy = ((C0 + rsub) * S + rg * 64) * 2 + 16 * chs;
#pragma unroll
    for (int i = 0; i < 2; ++i) { const int blk = w + 4 * i; dma16(ldsb + LF_B + blk * 1024, vy, rsy, blk * 8 * S * 2); }
  }
  u32x4 of[16];
  {
    const int rt16 = R0 >> 4;
    const u32x4* src = (const u32x4*)(p.ws + WS_OF) + (size_t)((rt16 >> 1) * 16 * 2 + (g >> 1)) * 64 + l16 + 16 * (rt16 & 1) + 32 * (g & 1);
#pragma unroll
    for (int hh = 0; hh < 16; ++hh) of[hh] = src[hh * 128];
  }
  const int l = R0 + l16;
  const float gg = ((const float*)(p.ws + WS_G))[l];
  asm volatile("" ::: "memory");
  WAITVM(17);
  bar();
  STAMP(1, 1);
  f32x4 acc[4];
#pragma unroll
  for (int ct = 0; ct < 4; ++ct) acc[ct] = (f32x4){0, 0, 0, 0};
  const int q = l16 >> 2, pp = lane & 3;
#pragma unroll
  for (int hh = 0; hh < 16; ++hh) {
    const int rowa = 28 * hh + 16 * (g >> 1) + 4 * (g & 1) + q, rowb = rowa + 8;
#pragma unroll
    for (int ct = 0; ct < 4; ++ct) {
      const u32x2 lo = tr_read(lds + rowa * 128 + ((ct ^ ((rowa >> 1) & 3)) << 5) + 8 * pp);
      const u32x2 hi = tr_read(lds + rowb * 128 + ((ct ^ ((rowb >> 1) & 3)) << 5) + 8 * pp);
      acc[ct] = mfma16(cat(lo, hi), of[hh], acc[ct]);
    }
  }
  const float g1 = rbf(1.f - gg);
#pragma unroll
  for (int ct = 0; ct < 4; ++ct) {
    const int crow_ = 16 * ct + 4 * g + q;
    const int c32 = (16 * w * 2) >> 5;
    const u32x2 bv = tr_read(lds + LF_B + crow_ * 128 + ((c32 ^ ((crow_ >> 1) & 3)) << 5) + 8 * pp);
    const float bs[4] = {bf2f(bv.x), bfhi(bv.x), bf2f(bv.y), bfhi(bv.y)};
    float o[4];
#pragma unroll
    for (int r = 0; r < 4; ++r) o[r] = rbf(gg * rbf(acc[ct][r])) + rbf(g1 * bs[r]);
    *(u32x2*)(p.out + (size_t)l * D + C0 + 16 * ct + 4 * g) = (u32x2){pk_bf16(o[0], o[1]), pk_bf16(o[2], o[3])};
  }
  STAMP(1, 2);
}

extern "C" void kernel_launch(void* const* d_in, const int* in_sizes, int n_in, void* d_out, int out_size, void* d_ws, size_t ws_size, hipStream_t stream) {
  Params p;
  p.x = (const u16*)d_in[0]; p.wqkv = (const u16*)d_in[1]; p.wphi = (const u16*)d_in[2]; p.wo = (const u16*)d_in[3];
  p.Are = (const u16*)d_in[5]; p.Aim = (const u16*)d_in[6]; p.Bre = (const u16*)d_in[7]; p.Bim = (const u16*)d_in[8];
  p.Cre = (const u16*)d_in[9]; p.Cim = (const u16*)d_in[10]; p.logdt = (const u16*)d_in[11]; p.dskip = (const u16*)d_in[12]; p.gatew = (const u16*)d_in[13];
  p.out = (u16*)d_out; p.ws = (char*)d_ws;
  (void)hipFuncSetAttribute((const void*)k_main, hipFuncAttributeMaxDynamicSharedMemorySize, LDS_BYTES);
  (void)hipFuncSetAttribute((const void*)k_fin, hipFuncAttributeMaxDynamicSharedMemorySize, LF_BYTES);
  k_main<<<512, 256, LDS_BYTES, stream>>>(p);
  k_fin<<<224, 256, LF_BYTES, stream>>>(p);
}
```

```cpp
#include <hip/hip_runtime.h>
#include <stdint.h>

typedef unsigned short u16;
typedef unsigned int u32;
typedef unsigned long long u64;
typedef short s16x4 __attribute__((ext_vector_type(4)));
typedef _Float16 f16x8 __attribute__((ext_vector_type(8)));
typedef _Float16 f16x2 __attribute__((ext_vector_type(2)));
typedef _Float16 f16x4 __attribute__((ext_vector_type(4)));
typedef __bf16 bf16x8 __attribute__((ext_vector_type(8)));
typedef __bf16 bf16x2 __attribute__((ext_vector_type(2)));
typedef float f32x16 __attribute__((ext_vector_type(16)));
typedef float f32x4 __attribute__((ext_vector_type(4)));
typedef float f32x2 __attribute__((ext_vector_type(2)));
typedef u32 u32x4 __attribute__((ext_vector_type(4)));
typedef u32 u32x2 __attribute__((ext_vector_type(2)));
typedef u16 u16x2 __attribute__((ext_vector_type(2)));
#define LAS __attribute__((address_space(3)))
#define DI __device__ __forceinline__
typedef __amdgpu_buffer_rsrc_t rsrc_t;

constexpr int S = 2048, D = 448, H = 16, DH = 28, NS = 256;
constexpr float LOG2E = 1.4426950408889634f;
constexpr float INV2PI = 0.15915494309189535f;

constexpr u32 WS_EPOCH = 0;
constexpr u32 WS_TMO   = 256;
constexpr u32 WS_SEEN  = 4096;
constexpr u32 WS_G     = 16384;
constexpr u32 WS_G2    = 64u << 10;
constexpr u32 WS_G1    = 1u << 20;
constexpr u32 WS_OF    = 3u << 20;
constexpr u32 WS_XT    = 5u << 20;
constexpr u32 WS_YB    = 9u << 20;
constexpr u32 WS_ST    = 13u << 20;
constexpr u32 WS_USED  = 16u << 20;
constexpr u32 SENT = 0xFFFFFFFFu;

#define TSX(i)
#define TS_DECL
#define TS(i)
#define TS_OUT()

struct Params {
  const u16 *x, *wqkv, *wphi, *wo, *Are, *Aim, *Bre, *Bim, *Cre, *Cim, *logdt, *dskip, *gatew;
  u16* out; char* ws;
};

DI u32 f2u(float v) { return __builtin_bit_cast(u32, v); }
DI float u2f(u32 v) { return __builtin_bit_cast(float, v); }
DI float bf2f(u32 v) { return __builtin_bit_cast(float, v << 16); }
DI float bfhi(u32 v) { return __builtin_bit_cast(float, v & 0xffff0000u); }
DI u32 pk_bf16(float a, float b) { f32x2 v = {a, b}; return __builtin_bit_cast(u32, __builtin_convertvector(v, bf16x2)); }
DI u32 pk_f16(float a, float b) { f32x2 v = {a, b}; return __builtin_bit_cast(u32, __builtin_convertvector(v, f16x2)); }
DI float rbf(float a) { return u2f(pk_bf16(0.f, a)); }
DI f32x4 mfma16(u32x4 a, u32x4 b, f32x4 c) { return __builtin_amdgcn_mfma_f32_16x16x32_bf16(__builtin_bit_cast(bf16x8, a), __builtin_bit_cast(bf16x8, b), c, 0, 0, 0); }
DI f32x16 mfma32(u32x4 a, u32x4 b, f32x16 c) { return __builtin_amdgcn_mfma_f32_32x32x16_bf16(__builtin_bit_cast(bf16x8, a), __builtin_bit_cast(bf16x8, b), c, 0, 0, 0); }
DI f32x4 mfma_h(u32x4 a, u32x4 b, f32x4 c) { return __builtin_amdgcn_mfma_f32_16x16x32_f16(__builtin_bit_cast(f16x8, a), __builtin_bit_cast(f16x8, b), c, 0, 0, 0); }
DI u32x2 tr_read(LAS char* p) { return __builtin_bit_cast(u32x2, __builtin_amdgcn_ds_read_tr16_b64_v4i16((LAS s16x4*)p)); }
DI u32x4 cat(u32x2 a, u32x2 b) { return (u32x4){a.x, a.y, b.x, b.y}; }
template <int CTRL> DI float dpp(float x) { return __builtin_bit_cast(float, __builtin_amdgcn_mov_dpp(__builtin_bit_cast(int, x), CTRL, 0xf, 0xf, true)); }
DI float row16_max(float x) { x = fmaxf(x, dpp<0xB1>(x)); x = fmaxf(x, dpp<0x4E>(x)); x = fmaxf(x, dpp<0x141>(x)); x = fmaxf(x, dpp<0x140>(x)); return x; }
DI float row16_sum(float x) { x += dpp<0xB1>(x); x += dpp<0x4E>(x); x += dpp<0x141>(x); x += dpp<0x140>(x); return x; }
DI void swap16(float x, float& a, float& b) { const int u = __builtin_bit_cast(int, x); auto r = __builtin_amdgcn_permlane16_swap(u, u, false, false); const int r0 = r[0], r1 = r[1]; a = __builtin_bit_cast(float, r0); b = __builtin_bit_cast(float, r1); }
DI void swap32(float x, float& a, float& b) { const int u = __builtin_bit_cast(int, x); auto r = __builtin_amdgcn_permlane32_swap(u, u, false, false); const int r0 = r[0], r1 = r[1]; a = __builtin_bit_cast(float, r0); b = __builtin_bit_cast(float, r1); }
DI float wave_max(float x) { float a, b; x = row16_max(x); swap16(x, a, b); x = fmaxf(a, b); swap32(x, a, b); return fmaxf(a, b); }
DI float wave_sum(float x) { float a, b; x = row16_sum(x); swap16(x, a, b); x = a + b; swap32(x, a, b); return a + b; }
DI float exp2f_(float x) { return __builtin_amdgcn_exp2f(x); }
DI float rcpf_(float x) { return __builtin_amdgcn_rcpf(x); }
DI rsrc_t mkrs(const void* p, u32 bytes) { return __builtin_amdgcn_make_buffer_rsrc((void*)p, 0, (int)bytes, 0x00020000); }
DI void dma16(u32 lds_addr, u32 voff, rsrc_t rs, u32 soff) {
  asm volatile("s_mov_b32 m0, %0\n\ts_nop 0\n\tbuffer_load_dwordx4 %1, %2, %3 offen lds" :: "s"(lds_addr), "v"(voff), "s"(rs), "s"(soff) : "memory");
}
template <int N> DI void waitvm() { asm volatile("s_waitcnt vmcnt(%0)" :: "n"(N) : "memory"); }
DI void bar() { asm volatile("s_waitcnt lgkmcnt(0)" ::: "memory"); __builtin_amdgcn_s_barrier(); asm volatile("" ::: "memory"); }
constexpr int AUX_SC1 = 16;
DI u32x4 ld16_sc1(rsrc_t rs, u32 off) { return __builtin_amdgcn_raw_buffer_load_b128(rs, off, 0, AUX_SC1); }
DI u32x2 ld8_sc1(rsrc_t rs, u32 off) { return __builtin_amdgcn_raw_buffer_load_b64(rs, off, 0, AUX_SC1); }
DI u32 ld4_sc1(rsrc_t rs, u32 off) { return __builtin_amdgcn_raw_buffer_load_b32(rs, off, 0, AUX_SC1); }
DI void st16_sc1(rsrc_t rs, u32 off, u32x4 v) { __builtin_amdgcn_raw_buffer_store_b128(v, rs, off, 0, AUX_SC1); }
DI void st8_sc1(rsrc_t rs, u32 off, u32x2 v) { __builtin_amdgcn_raw_buffer_store_b64(v, rs, off, 0, AUX_SC1); }
DI void st4_sc1(rsrc_t rs, u32 off, u32 v) { __builtin_amdgcn_raw_buffer_store_b32(v, rs, off, 0, AUX_SC1); }
DI void ald16(u32x4& d, rsrc_t rs, u32 voff, u32 soff) { asm volatile("buffer_load_dwordx4 %0, %1, %2, %3 offen" : "=v"(d) : "v"(voff), "s"(rs), "s"(soff) : "memory"); }
DI void ald16s(u32x4& d, rsrc_t rs, u32 voff, u32 soff) { asm volatile("buffer_load_dwordx4 %0, %1, %2, %3 offen sc1" : "=v"(d) : "v"(voff), "s"(rs), "s"(soff) : "memory"); }
DI void ald8s(u32x2& d, rsrc_t rs, u32 voff, u32 soff) { asm volatile("buffer_load_dwordx2 %0, %1, %2, %3 offen sc1" : "=v"(d) : "v"(voff), "s"(rs), "s"(soff) : "memory"); }
template <int N> DI void waitv(u32x4& a) { asm volatile("s_waitcnt vmcnt(%1)" : "+v"(a) : "n"(N) : "memory"); }
template <int N> DI void waitv4(u32x4& a, u32x4& b, u32x4& c, u32x4& d) { asm volatile("s_waitcnt vmcnt(%4)" : "+v"(a), "+v"(b), "+v"(c), "+v"(d) : "n"(N) : "memory"); }
DI void tie(u32x4& a) { asm volatile("" : "+v"(a) :: "memory"); }
DI u32 max4(u32x4 v) { return max(max(v[0], v[1]), max(v[2], v[3])); }
DI u32 sane(u32 v) { return min(v, 0xFFFFFFFEu); }
constexpr u32 SPIN_CAP = 1u << 14;
DI bool spin_fail(u32& spins, rsrc_t rsws) {
  __builtin_amdgcn_s_sleep(1);
  asm volatile("" ::: "memory");
  if (++spins > SPIN_CAP) { st4_sc1(rsws, WS_TMO, 1u); return true; }
  return false;
}

constexpr int LDS_BYTES = 75776;

constexpr int LA_WK = 0, LA_WV = 28672, LA_XS = 57344, LA_WP = 73728;
constexpr int LA_WQ = LA_WK, LA_V = LA_WV, LA_WO = 0;
#define LOOKA 3
struct ASeq { int w[7]; };
constexpr ASeq make_aseq() {
  ASeq r{};
  int seq[128] = {}; int n = 0;
  for (int k = 0; k < LOOKA && k < 7; ++k) for (int e = 0; e < 6; ++e) seq[n++] = k;
  for (int c = 0; c < 7; ++c) {
    int last = -1;
    for (int i = 0; i < n; ++i) if (seq[i] == c) last = i;
    r.w[c] = n - 1 - last;
    if (c + LOOKA < 7) for (int e = 0; e < 6; ++e) seq[n++] = c + LOOKA;
    if (c >= 2) seq[n++] = -1;
  }
  return r;
}
constexpr ASeq ASEQ = make_aseq();

DI void softmax32(const f32x16& pin, bool hi, u32x4 (&fp)[2], u32x4 (&fn)[2]) {
  float t[16];
#pragma unroll
  for (int i = 0; i < 16; ++i) t[i] = rbf(pin[i]);
  float tp[4], tn[4];
#pragma unroll
  for (int i = 0; i < 4; ++i) { tp[i] = hi ? -__builtin_inff() : t[12 + i]; tn[i] = hi ? __builtin_inff() : t[12 + i]; }
  float mx = fmaxf(fmaxf(t[0], t[1]), t[2]), mn = fminf(fminf(t[0], t[1]), t[2]);
#pragma unroll
  for (int i = 3; i < 11; i += 2) { mx = fmaxf(fmaxf(mx, t[i]), t[i + 1]); mn = fminf(fminf(mn, t[i]), t[i + 1]); }
  mx = fmaxf(fmaxf(mx, t[11]), tp[0]); mx = fmaxf(fmaxf(mx, tp[1]), tp[2]); mx = fmaxf(mx, tp[3]);
  mn = fminf(fminf(mn, t[11]), tn[0]); mn = fminf(fminf(mn, tn[1]), tn[2]); mn = fminf(mn, tn[3]);
  { float a, b; swap32(mx, a, b); mx = fmaxf(a, b); swap32(mn, a, b); mn = fminf(a, b); }
  const f32x2 L2 = {LOG2E, LOG2E}, NL2 = {-LOG2E, -LOG2E};
  const f32x2 mxs = (f32x2){mx, mx} * NL2, mns = (f32x2){mn, mn} * L2;
  float ep[16], en[16];
#pragma unroll
  for (int i = 0; i < 16; i += 2) {
    const f32x2 a = i < 12 ? (f32x2){t[i], t[i + 1]} : (f32x2){tp[i - 12], tp[i - 11]};
    const f32x2 b = i < 12 ? (f32x2){t[i], t[i + 1]} : (f32x2){tn[i - 12], tn[i - 11]};
    const f32x2 xp = __builtin_elementwise_fma(a, L2, mxs), xn = __builtin_elementwise_fma(b, NL2, mns);
    ep[i] = exp2f_(xp[0]); ep[i + 1] = exp2f_(xp[1]); en[i] = exp2f_(xn[0]); en[i + 1] = exp2f_(xn[1]);
  }
  f32x2 sp2 = {ep[0], ep[1]}, sn2 = {en[0], en[1]};
#pragma unroll
  for (int i = 2; i < 16; i += 2) { sp2 += (f32x2){ep[i], ep[i + 1]}; sn2 += (f32x2){en[i], en[i + 1]}; }
  float sp = sp2[0] + sp2[1], sn = sn2[0] + sn2[1];
  { float a, b; swap32(sp, a, b); sp = a + b; swap32(sn, a, b); sn = a + b; }
  const float ip = rcpf_(sp), in = rcpf_(sn);
#pragma unroll
  for (int s = 0; s < 2; ++s)
#pragma unroll
    for (int k = 0; k < 4; ++k) {
      const f32x2 a = (f32x2){ep[8 * s + 2 * k], ep[8 * s + 2 * k + 1]} * (f32x2){ip, ip};
      const f32x2 b = (f32x2){en[8 * s + 2 * k], en[8 * s + 2 * k + 1]} * (f32x2){in, in};
      fp[s][k] = pk_bf16(a[0], a[1]);
      fn[s][k] = pk_bf16(b[0], b[1]);
    }
}
DI u32x4 pack8(const f32x16& a, int s) { return (u32x4){pk_bf16(a[8 * s], a[8 * s + 1]), pk_bf16(a[8 * s + 2], a[8 * s + 3]), pk_bf16(a[8 * s + 4], a[8 * s + 5]), pk_bf16(a[8 * s + 6], a[8 * s + 7])}; }

DI void check_seen(u32x4 v, u32 tag, rsrc_t rsws, int lane) {
  u32 spins = 0;
#pragma unroll 1
  while (!__all((v[0] == tag) & (v[1] == tag) & (v[2] == tag) & (v[3] == tag))) {
    if (spin_fail(spins, rsws)) break;
    v = ld16_sc1(rsws, WS_SEEN + lane * 16);
  }
}

DI void roleA(const Params& p, int xcd, int o, LAS char* lds, u32 ev, rsrc_t rsws) {
  TS_DECL;
  TS(0);
  const int tid = threadIdx.x, lane = tid & 63, w = __builtin_amdgcn_readfirstlane(tid >> 6);
  const int h = 8 * (xcd & 1) + (o & 7), rr = 4 * (xcd >> 1) + (o >> 3);
  const int l32 = lane & 31, hh = lane >> 5, l16 = lane & 15, g = lane >> 4;
  const bool hi = lane >= 32;
  __builtin_amdgcn_s_setprio(2);
  const u32 ldsb = (u32)(uintptr_t)lds;
  const rsrc_t rsW = mkrs(p.wqkv, D * 3 * D * 2), rsP = mkrs(p.wphi, H * DH * DH * 2), rsX = mkrs(p.x, S * D * 2);
  const u32 wvoff = ((lane >> 2) * (3 * D) + h * DH) * 2 + (lane & 3) * 16;
  const int r0 = rr * 128 + w * 32;
  int c0 = 2 * (rr & 3) + (h & 7); c0 = c0 >= 7 ? c0 - 7 : c0; c0 = c0 >= 7 ? c0 - 7 : c0;
  u32x4 xr[7][4];
  const u32 xvoff = (r0 + (lane >> 3)) * (D * 2) + 16 * (lane & 7);
  const u32 wv2 = wvoff + (1 + (w >> 1)) * (D * 2);
  const u32 lw = ldsb + LA_WK + (w >> 1) * 28672 + (w & 1) * 2048;
  const u32 so = (w & 1) * 32 * (3 * D * 2);
  auto chunk_of = [&](int i) { int c = c0 + i; return c >= 7 ? c - 7 : c; };
  auto issue = [&](int i) {
    const u32 c = chunk_of(i);
    dma16(lw + i * 4096, wv2, rsW, so + c * 64 * (3 * D * 2));
    dma16(lw + i * 4096 + 1024, wv2, rsW, so + c * 64 * (3 * D * 2) + 16 * (3 * D * 2));
#pragma unroll
    for (int k = 0; k < 4; ++k) ald16(xr[i][k], rsX, xvoff, c * 128 + k * 8 * D * 2);
  };
  dma16(ldsb + LA_WP + (w & 1) * 1024, (h * (DH * DH) + ((w & 1) * 16 + (lane >> 2)) * DH) * 2 + (lane & 3) * 16, rsP, 0);
#pragma unroll
  for (int i = 0; i < LOOKA; ++i) issue(i);
  TS(1);
  LAS char* xs = lds + LA_XS + w * 4096;
  const int r3 = lane >> 3, ch8 = lane & 7;
  LAS char* wa0 = xs + r3 * 128 + ((ch8 ^ (r3 >> 1)) << 4);
  LAS char* wa1 = xs + r3 * 128 + ((ch8 ^ (r3 >> 1) ^ 4) << 4);
  LAS char* ra[4];
#pragma unroll
  for (int j = 0; j < 4; ++j) ra[j] = xs + l32 * 128 + (((2 * j + hh) ^ ((l32 >> 1) & 7)) << 4);
  LAS char* wb = lds + (8 * hh + ((lane & 15) >> 2)) * 64 + 32 * ((lane >> 4) & 1) + 8 * (lane & 3);
  u32x4 xf[28];
  f32x16 ak, av;
#pragma unroll
  for (int i = 0; i < 16; ++i) { ak[i] = 0.f; av[i] = 0.f; }
  u32x4 wk[2][4], wv[2][4];
  u32 tag = 0;
#pragma unroll
  for (int c = 0; c < 8; ++c) {
    if (c < 7) {
      if (c == 0) waitv4<ASEQ.w[0]>(xr[0][0], xr[0][1], xr[0][2], xr[0][3]);
      else if (c == 1) waitv4<ASEQ.w[1]>(xr[1][0], xr[1][1], xr[1][2], xr[1][3]);
      else if (c == 2) waitv4<ASEQ.w[2]>(xr[2][0], xr[2][1], xr[2][2], xr[2][3]);
      else if (c == 3) waitv4<ASEQ.w[3]>(xr[3][0], xr[3][1], xr[3][2], xr[3][3]);
      else if (c == 4) waitv4<ASEQ.w[4]>(xr[4][0], xr[4][1], xr[4][2], xr[4][3]);
      else if (c == 5) waitv4<ASEQ.w[5]>(xr[5][0], xr[5][1], xr[5][2], xr[5][3]);
      else waitv4<ASEQ.w[6]>(xr[6][0], xr[6][1], xr[6][2], xr[6][3]);
      __builtin_amdgcn_s_barrier();
      asm volatile("" ::: "memory");
      if (c == 0) TS(2);
      if (c == 3) TS(3);
      *(LAS u32x4*)(wa0) = xr[c][0]; *(LAS u32x4*)(wa1 + 1024) = xr[c][1]; *(LAS u32x4*)(wa0 + 2048) = xr[c][2]; *(LAS u32x4*)(wa1 + 3072) = xr[c][3];
#pragma unroll
      for (int j = 0; j < 4; ++j) {
        const int ks = 4 * c + j;
        xf[ks] = *(LAS u32x4*)(ra[j]);
        wk[c & 1][j] = cat(tr_read(wb + LA_WK + ks * 1024), tr_read(wb + LA_WK + ks * 1024 + 256));
        wv[c & 1][j] = cat(tr_read(wb + LA_WV + ks * 1024), tr_read(wb + LA_WV + ks * 1024 + 256));
      }
    }
    if (c > 0) {
#pragma unroll
      for (int j = 0; j < 4; ++j) {
        const int ks = 4 * (c - 1) + j;
        ak = mfma32(wk[(c - 1) & 1][j], xf[ks], ak);
        av = mfma32(wv[(c - 1) & 1][j], xf[ks], av);
      }
    }
    if (c < 7) {
#pragma unroll
      for (int j = 0; j < 8; ++j) { __builtin_amdgcn_sched_group_barrier(0x300, 3, 0); if (c > 0) __builtin_amdgcn_sched_group_barrier(0x8, 1, 0); }
    }
    __builtin_amdgcn_sched_barrier(0);
    if (c + LOOKA < 7) issue(c + LOOKA);
    if (c >= 2 && c < 7) dma16(ldsb + LA_WQ + (c - 2) * 4096 + w * 1024, wvoff, rsW, (chunk_of(c - 2) * 64 + w * 16) * (3 * D * 2));
  }
  TS(4);
  u32x4 wf[2];
  {
    LAS char* pb = lds + LA_WP + (4 * hh + ((lane & 15) >> 2)) * 64 + 32 * ((lane >> 4) & 1) + 8 * (lane & 3);
#pragma unroll
    for (int s = 0; s < 2; ++s) wf[s] = cat(tr_read(pb + s * 1024), tr_read(pb + s * 1024 + 512));
  }
  bar();
#pragma unroll
  for (int i = 5; i < 7; ++i) dma16(ldsb + LA_WQ + i * 4096 + w * 1024, wvoff, rsW, (chunk_of(i) * 64 + w * 16) * (3 * D * 2));
  u32x4 seen;
  ald16s(seen, rsws, lane * 16, WS_SEEN);
  f32x16 z16;
#pragma unroll
  for (int i = 0; i < 16; ++i) z16[i] = 0.f;
  {
    u32x4 k0 = pack8(ak, 0), k1 = pack8(ak, 1);
    if (hi) { k1[2] = 0; k1[3] = 0; }
    f32x16 pk = mfma32(wf[0], k0, z16);
    pk = mfma32(wf[1], k1, pk);
    const int sw = (l32 >> 1) & 7;
    LAS char* vt = lds + LA_V + w * 2048 + l32 * 64;
    {
      u32x4 v0 = pack8(av, 0), v1 = pack8(av, 1);
      if (hi) { v1[2] = 0x00003f80u; v1[3] = 0; }
      *(LAS u32x2*)(vt + 8 * ((hh) ^ sw)) = (u32x2){v0[0], v0[1]};
      *(LAS u32x2*)(vt + 8 * ((2 + hh) ^ sw)) = (u32x2){v0[2], v0[3]};
      *(LAS u32x2*)(vt + 8 * ((4 + hh) ^ sw)) = (u32x2){v1[0], v1[1]};
      *(LAS u32x2*)(vt + 8 * ((6 + hh) ^ sw)) = (u32x2){v1[2], v1[3]};
    }
    u32x4 fp[2], fn[2];
    softmax32(pk, hi, fp, fn);
#pragma unroll
    for (int s = 0; s < 2; ++s) {
      *(LAS u32x2*)(xs + l32 * 64 + 8 * ((4 * s + hh) ^ sw)) = (u32x2){fp[s][0], fp[s][1]};
      *(LAS u32x2*)(xs + l32 * 64 + 8 * ((4 * s + 2 + hh) ^ sw)) = (u32x2){fp[s][2], fp[s][3]};
      *(LAS u32x2*)(xs + 2048 + l32 * 64 + 8 * ((4 * s + hh) ^ sw)) = (u32x2){fn[s][0], fn[s][1]};
      *(LAS u32x2*)(xs + 2048 + l32 * 64 + 8 * ((4 * s + 2 + hh) ^ sw)) = (u32x2){fn[s][2], fn[s][3]};
    }
  }
  TS(5);
  waitv<0>(seen);
  asm volatile("" : "+v"(ev));
  tag = (u32)__builtin_amdgcn_readfirstlane(ev) + 1u;
  check_seen(seen, tag, rsws, lane);
  bar();
  {
    const int q = l16 >> 2, pp = lane & 3;
    const int ra_ = 8 * g + q, rb_ = ra_ + 4;
    const int sa = (ra_ >> 1) & 7, sb = (rb_ >> 1) & 7;
    LAS char* pa = lds + LA_XS + (w >> 1) * 2048;
    const int ua = 4 * (w & 1) + pp;
    f32x4 kv0 = {0, 0, 0, 0}, kv1 = {0, 0, 0, 0};
#pragma unroll
    for (int kt = 0; kt < 4; ++kt) {
      const u32x4 A = cat(tr_read(pa + kt * 4096 + ra_ * 64 + 8 * (ua ^ sa)), tr_read(pa + kt * 4096 + rb_ * 64 + 8 * (ua ^ sb)));
      LAS char* vb = lds + LA_V + kt * 2048;
      const u32x4 B0 = cat(tr_read(vb + ra_ * 64 + 8 * (pp ^ sa)), tr_read(vb + rb_ * 64 + 8 * (pp ^ sb)));
      const u32x4 B1 = cat(tr_read(vb + ra_ * 64 + 8 * ((4 + pp) ^ sa)), tr_read(vb + rb_ * 64 + 8 * ((4 + pp) ^ sb)));
      kv0 = mfma16(A, B0, kv0);
      kv1 = mfma16(A, B1, kv1);
    }
    const u32 base = WS_G1 + (u32)((h * 16 + rr) * 8 + 2 * w) * 1024 + lane * 16;
    st16_sc1(rsws, base, (u32x4){sane(f2u(kv0[0])), sane(f2u(kv0[1])), sane(f2u(kv0[2])), sane(f2u(kv0[3]))});
    st16_sc1(rsws, base + 1024, (u32x4){sane(f2u(kv1[0])), sane(f2u(kv1[1])), sane(f2u(kv1[2])), sane(f2u(kv1[3]))});
  }
  if (tid == 192) st4_sc1(rsws, WS_SEEN + 1024 + (u32)(o * 8 + xcd) * 4, tag);
  TS(6);
  const bool reducer = (w == 0) && (rr < 8);
  u32x4 pv[16];
  const u32 g1off = WS_G1 + (u32)(h * 16 * 8 + rr) * 1024;
  if (reducer) {
#pragma unroll
    for (int s = 0; s < 16; ++s) ald16s(pv[s], rsws, lane * 16, g1off + s * 8192);
  }
  f32x16 aq = z16;
#pragma unroll
  for (int ks = 0; ks < 28; ++ks) {
    const u32x4 wq = cat(tr_read(wb + LA_WQ + ks * 1024), tr_read(wb + LA_WQ + ks * 1024 + 256));
    aq = mfma32(wq, xf[ks], aq);
  }
  bar();
  const bool doF = o < 28;
  const int frg = 4 * xcd + o / 7, fcg = o % 7;
  const int C0 = 64 * fcg;
  if (doF && w > 0) {
    const int rsub = lane >> 3, pos = lane & 7;
    const int chs = (((pos >> 1) ^ ((rsub >> 1) & 3)) << 1) | (pos & 1);
    const rsrc_t rs = mkrs(p.wo, D * D * 2);
    const u32 voff = (rsub * D + C0) * 2 + 16 * chs;
#pragma unroll
    for (int i = 0; i < 19; ++i) { const int blk = (w - 1) + 3 * i; dma16(ldsb + LA_WO + blk * 1024, voff, rs, blk * 8 * D * 2); }
  }
  TSX(7);
  if (reducer) {
    waitv4<0>(pv[0], pv[1], pv[2], pv[3]);
#pragma unroll
    for (int s = 4; s < 16; ++s) tie(pv[s]);
    u32 spins = 0;
#pragma unroll 1
    for (;;) {
      u32 m = 0;
#pragma unroll
      for (int s = 0; s < 16; ++s) m = max(m, max4(pv[s]));
      if (__all(m != SENT) || spin_fail(spins, rsws)) break;
#pragma unroll
      for (int s = 0; s < 16; ++s) pv[s] = ld16_sc1(rsws, g1off + s * 8192 + lane * 16);
    }
    f32x4 a = {0, 0, 0, 0};
#pragma unroll
    for (int s = 0; s < 16; ++s) { const u32 x0 = pv[s][0], x1 = pv[s][1], x2 = pv[s][2], x3 = pv[s][3]; a += (f32x4){u2f(x0), u2f(x1), u2f(x2), u2f(x3)}; }
    st8_sc1(rsws, WS_G2 + (u32)h * 4096 + (u32)rr * 512 + (u32)((l16 * 2 + (g & 1)) * 2 + (g >> 1)) * 8, (u32x2){sane(pk_bf16(a[0], a[1])), sane(pk_bf16(a[2], a[3]))});
  }
  TSX(8);
  u32x4 fp[2], fn[2];
  {
    u32x4 q0 = pack8(aq, 0), q1 = pack8(aq, 1);
    if (hi) { q1[2] = 0; q1[3] = 0; }
    f32x16 pq = mfma32(wf[0], q0, z16);
    pq = mfma32(wf[1], q1, pq);
    softmax32(pq, hi, fp, fn);
  }
  TSX(9);
  {
    u32x4 kvf[4];
    const u32 voff = (u32)(l32 >> 4) * 512 + (u32)((l32 & 15) * 2 + hh) * 16;
    u32 spins = 0;
#pragma unroll 1
    for (;;) {
#pragma unroll
      for (int f = 0; f < 4; ++f) kvf[f] = ld16_sc1(rsws, WS_G2 + (u32)h * 4096 + f * 1024 + voff);
      const u32 m = max(max(max4(kvf[0]), max4(kvf[1])), max(max4(kvf[2]), max4(kvf[3])));
      if (__all(m != SENT) || spin_fail(spins, rsws)) break;
    }
    waitvm<0>();
    TSX(10);
    f32x16 n = mfma32(kvf[0], fp[0], z16);
    f32x16 n2 = mfma32(kvf[2], fn[0], z16);
    n = mfma32(kvf[1], fp[1], n);
    n2 = mfma32(kvf[3], fn[1], n2);
    n += n2;
    float a, b; swap32(n[12], a, b);
    const float id = rcpf_(rbf(rbf(b) + 1e-6f));
    u32x4 of[2];
#pragma unroll
    for (int s = 0; s < 2; ++s)
#pragma unroll
      for (int k = 0; k < 4; ++k) of[s][k] = sane(pk_bf16(rbf(n[8 * s + 2 * k]) * id, rbf(n[8 * s + 2 * k + 1]) * id));
    if (hi) { of[1][2] = 0; of[1][3] = 0; }
    const u32 ob = WS_OF + (u32)(((r0 >> 5) * 16 + h) * 2) * 1024 + lane * 16;
    st16_sc1(rsws, ob, of[0]); st16_sc1(rsws, ob + 1024, of[1]);
  }
  TSX(11);
  if (doF) {
    bar();
    const int R0 = frg * 64 + 16 * w;
    const int rt16 = R0 >> 4;
    u32x4 of[16];
    const u32 ovoff = (u32)(((rt16 >> 1) * 16 * 2 + (g >> 1)) * 64 + l16 + 16 * (rt16 & 1) + 32 * (g & 1)) * 16;
#pragma unroll
    for (int i = 0; i < 16; ++i) ald16s(of[i], rsws, ovoff, WS_OF + i * 2048);
    const int l = R0 + l16;
    u32x2 gv, bv[8];
    const u32 bvoff = (u32)(C0 / 2 + 2 * g) * 16384 + (u32)(l >> 8) * 2048 + (u32)((l16 >> 1) & 1) * 1024 + (u32)((l >> 4) & 15) * 64 + (u32)(l16 >> 2) * 16 + (u32)(l16 & 1) * 8;
    ald8s(gv, rsws, (u32)l * 8, WS_G);
#pragma unroll
    for (int ct = 0; ct < 4; ++ct) { ald8s(bv[2 * ct], rsws, bvoff, WS_YB + ct * 8 * 16384); ald8s(bv[2 * ct + 1], rsws, bvoff, WS_YB + (ct * 8 + 1) * 16384); }
    f32x4 acc[4];
#pragma unroll
    for (int ct = 0; ct < 4; ++ct) acc[ct] = (f32x4){0, 0, 0, 0};
    const int q = l16 >> 2, pp = lane & 3;
#define FSTEP(i) { \
      waitv<15 - (i) + 9>(of[i]); \
      if (__builtin_expect(__any(max4(of[i]) == SENT), 0)) { \
        u32 spins = 0; \
        _Pragma("unroll 1") do { if (spin_fail(spins, rsws)) break; of[i] = ld16_sc1(rsws, WS_OF + (i) * 2048 + ovoff); } while (__any(max4(of[i]) == SENT)); \
      } \
      const int rowa = 28 * (i) + 16 * (g >> 1) + 4 * (g & 1) + q, rowb = rowa + 8; \
      _Pragma("unroll") for (int ct = 0; ct < 4; ++ct) { \
        const u32x2 lo = tr_read(lds + LA_WO + rowa * 128 + ((ct ^ ((rowa >> 1) & 3)) << 5) + 8 * pp); \
        const u32x2 hi2 = tr_read(lds + LA_WO + rowb * 128 + ((ct ^ ((rowb >> 1) & 3)) << 5) + 8 * pp); \
        acc[ct] = mfma16(cat(lo, hi2), of[i], acc[ct]); \
      } }
    FSTEP(0) FSTEP(1) FSTEP(2) FSTEP(3) FSTEP(4) FSTEP(5) FSTEP(6) FSTEP(7)
    FSTEP(8) FSTEP(9) FSTEP(10) FSTEP(11) FSTEP(12) FSTEP(13) FSTEP(14) FSTEP(15)
    TS(12);
    asm volatile("s_waitcnt vmcnt(0)" : "+v"(gv), "+v"(bv[0]), "+v"(bv[1]), "+v"(bv[2]), "+v"(bv[3]), "+v"(bv[4]), "+v"(bv[5]), "+v"(bv[6]), "+v"(bv[7]) :: "memory");
    {
      u32 spins = 0;
#pragma unroll 1
      for (;;) {
        bool ok = gv[1] == tag;
#pragma unroll
        for (int i = 0; i < 8; ++i) ok &= bv[i][1] == tag;
        if (__all(ok) || spin_fail(spins, rsws)) break;
        gv = ld8_sc1(rsws, WS_G + (u32)l * 8);
#pragma unroll
        for (int ct = 0; ct < 4; ++ct) { bv[2 * ct] = ld8_sc1(rsws, WS_YB + ct * 8 * 16384 + bvoff); bv[2 * ct + 1] = ld8_sc1(rsws, WS_YB + (ct * 8 + 1) * 16384 + bvoff); }
      }
    }
    TS(13);
    const u32 gu = gv[0];
    const float gg = u2f(gu);
    const float g1 = rbf(1.f - gg);
#pragma unroll
    for (int ct = 0; ct < 4; ++ct) {
      const u32 b01 = bv[2 * ct][0], b23 = bv[2 * ct + 1][0];
      const float bs[4] = {bf2f(b01), bfhi(b01), bf2f(b23), bfhi(b23)};
      float ov[4];
#pragma unroll
      for (int r = 0; r < 4; ++r) ov[r] = rbf(gg * rbf(acc[ct][r])) + rbf(g1 * bs[r]);
      *(u32x2*)(p.out + (size_t)l * D + C0 + 16 * ct + 4 * g) = (u32x2){pk_bf16(ov[0], ov[1]), pk_bf16(ov[2], ov[3])};
    }
  }
  TS(14);
  TS_OUT();
}

DI void init_sentinels(int xcd, int o, rsrc_t rsws) {
  const int tid = threadIdx.x, lane = tid & 63, w = tid >> 6;
  const int h = 8 * (xcd & 1) + (o & 7), rr = 4 * (xcd >> 1) + (o >> 3);
  const u32x4 s4 = {SENT, SENT, SENT, SENT};
  const u32 b1 = WS_G1 + (u32)(h * 16 + rr) * 8192 + (u32)tid * 16;
  st16_sc1(rsws, b1, s4); st16_sc1(rsws, b1 + 4096, s4);
  const u32 b2 = WS_OF + (u32)(((rr * 4 + w) * 16 + h) * 2) * 1024 + (u32)lane * 16;
  st16_sc1(rsws, b2, s4); st16_sc1(rsws, b2 + 1024, s4);
  if (tid < 16) st16_sc1(rsws, WS_G2 + (u32)h * 4096 + (u32)rr * 256 + (u32)tid * 16, s4);
}

constexpr int CPS = 4160;
constexpr int CPCH = 4 * CPS;
constexpr int LB_CP = 0;
constexpr int LB_U = 2 * CPCH;
constexpr int LB_KC = LB_U + 2 * 4608;
constexpr int LB_MISC = LB_KC + 2 * 1536;
constexpr int LB_EX = LB_MISC + 256;
constexpr int LB_STG = 0;
constexpr int LB_KT = LB_EX;
constexpr int LB_TT = LB_EX + 12288;
static_assert(LB_EX + 24576 <= LDS_BYTES, "lds");
static_assert(LB_EX % 16 == 0, "lds");
struct cpx2 { f32x2 re, im; };
DI cpx2 cmul(const cpx2& a, const cpx2& b) { cpx2 r; r.re = a.re * b.re - a.im * b.im; r.im = a.re * b.im + a.im * b.re; return r; }
DI cpx2 csqr(const cpx2& a) { cpx2 r; r.re = a.re * a.re - a.im * a.im; r.im = (a.re + a.re) * a.im; return r; }
DI cpx2 cexp_m(f32x2 al, f32x2 th, float m) {
  cpx2 r;
#pragma unroll
  for (int i = 0; i < 2; ++i) {
    const float mag = exp2f_(al[i] * m);
    const float ph = __builtin_amdgcn_fractf(th[i] * m);
    r.re[i] = mag * __builtin_amdgcn_cosf(ph); r.im[i] = mag * __builtin_amdgcn_sinf(ph);
  }
  return r;
}
DI void put_row(LAS char* stg, int i, const cpx2& v) { *(LAS u32x2*)(stg + i * 528) = (u32x2){pk_f16(v.re[0], v.im[0]), pk_f16(v.re[1], v.im[1])}; }
DI void put_rowc(LAS char* stg, int i, const cpx2& v) { *(LAS u32x2*)(stg + i * 528) = (u32x2){pk_f16(v.re[0], -v.im[0]), pk_f16(v.re[1], -v.im[1])}; }
DI float pow2_scale(float mx, int target) {
  const int e = (int)((__builtin_bit_cast(u32, mx) >> 23) & 0xff);
  int se = 254 + target - e;
  se = se < 1 ? 1 : (se > 254 ? 254 : se);
  return __builtin_bit_cast(float, (u32)se << 23);
}
DI u32 kpos(int l) {
  const u32 r = (pk_bf16((float)l, 0.f) & 0xffffu) - 17024u;
  const u32 v = l < 128 ? (u32)l : r;
  return (l < 0 ? 641u : v) * 2u;
}

DI void roleB(const Params& p, int xcd, int o, LAS char* lds, u32 ev, rsrc_t rsws) {
  TS_DECL;
  TS(0);
  const int tid = threadIdx.x, lane = tid & 63, w = __builtin_amdgcn_readfirstlane(tid >> 6);
  const int l16 = lane & 15, g = lane >> 4;
  const int pair = xcd * 28 + o;
  const int d0 = 2 * pair;
  const int ch = w >> 1, half = w & 1, d = d0 + ch;
  LAS float* misc = (LAS float*)(lds + LB_MISC);
  const int tt_ = (xcd & 1) * 28 + o;
  const int trt = 8 * (xcd >> 1) + tt_ / 7, tcg = tt_ % 7;
  u32x4 tx[2];
#pragma unroll
  for (int i = 0; i < 2; ++i) tx[i] = *(const u32x4*)(p.x + (size_t)(64 * trt + 32 * i + (tid >> 3)) * D + 64 * tcg + 8 * (tid & 7));
  const int pi = (d * NS + half * 128 + 2 * lane) >> 1;
  const u32 are = ((const u32*)p.Are)[pi], aim = ((const u32*)p.Aim)[pi], bre = ((const u32*)p.Bre)[pi], bim = ((const u32*)p.Bim)[pi], cre = ((const u32*)p.Cre)[pi], cim = ((const u32*)p.Cim)[pi];
  const float ldt = bf2f(p.logdt[d]);
  const u32 dsk2 = ((const u32*)p.dskip)[pair];
  init_sentinels(xcd, o, rsws);
  u32 ko[11];
#pragma unroll
  for (int e = 0; e < 11; ++e) ko[e] = LB_KC + kpos(2047 - 8 * tid - e);
  TS(1);
  u32 tag;
  {
    LAS char* tt = lds + LB_TT;
#pragma unroll
    for (int i = 0; i < 2; ++i) { const int row = 32 * i + (tid >> 3); *(LAS u32x4*)(tt + row * 128 + (((tid & 7) ^ (row & 7)) << 4)) = tx[i]; }
    asm volatile("s_waitcnt vmcnt(0)" : "+v"(ev) :: "memory");
    tag = (u32)__builtin_amdgcn_readfirstlane(ev) + 1u;
    bar();
    if (tid == 0) st4_sc1(rsws, WS_SEEN + (u32)(o * 8 + xcd) * 4, tag);
    const int q = l16 >> 2, pp = lane & 3;
#pragma unroll
    for (int i = 0; i < 2; ++i) {
      const int lc = g + 4 * i;
      const int ra = 8 * lc + q, rb = ra + 4;
      const int chunk = 2 * w + (pp >> 1), sub = (pp & 1) * 8;
      const u32x2 lo = tr_read(tt + ra * 128 + ((chunk ^ (ra & 7)) << 4) + sub);
      const u32x2 hi = tr_read(tt + rb * 128 + ((chunk ^ (rb & 7)) << 4) + sub);
      const u32 a0 = WS_XT + (u32)(64 * tcg + 16 * w + l16) * 8192 + (u32)trt * 256 + (u32)(8 * i + g) * 16;
      st16_sc1(rsws, a0, (u32x4){lo.x, tag, lo.y, tag});
      st16_sc1(rsws, a0 + 64, (u32x4){hi.x, tag, hi.y, tag});
    }
  }
  u32x4 pf[4];
  {
    const rsrc_t rs = mkrs(p.wo, D * D * 2);
#pragma unroll
    for (int i = 0; i < 4; ++i) ald16(pf[i], rs, (u32)(tid + 256 * i) * 16 + ((tid + 256 * i) < 896 ? 0u : 0x40000000u), (u32)o * 14336);
  }
  TS(2);
  const float dt = rbf(__expf(ldt));
  const f32x2 al = (f32x2){bf2f(are), bfhi(are)} * (dt * LOG2E);
  const f32x2 th = (f32x2){bf2f(aim), bfhi(aim)} * dt * INV2PI;
  const f32x2 br = (f32x2){bf2f(bre), bfhi(bre)} * dt, bi = (f32x2){bf2f(bim), bfhi(bim)} * dt;
  const f32x2 cr = {bf2f(cre), bfhi(cre)}, ci = {bf2f(cim), bfhi(cim)};
  cpx2 W; W.re = cr * br - ci * bi; W.im = cr * bi + ci * br;
  const float wm0 = __builtin_amdgcn_sqrtf(W.re[0] * W.re[0] + W.im[0] * W.im[0]) * exp2f_(fmaxf(0.f, al[0] * 2048.f));
  const float wm1 = __builtin_amdgcn_sqrtf(W.re[1] * W.re[1] + W.im[1] * W.im[1]) * exp2f_(fmaxf(0.f, al[1] * 2048.f));
  const float wmax = wave_max(fmaxf(wm0, wm1)), wsum = wave_sum(wm0 + wm1);
  const float SR = pow2_scale(wmax, 14);
  W.re *= SR; W.im *= SR;
  LAS char* stgr = lds + LB_STG + w * 8448;
  LAS char* stg = stgr + lane * 8;
  LAS char* frd = stgr + l16 * 528 + 16 * g;
  const cpx2 Z8 = cexp_m(al, th, 8.f);
  const cpx2 Z16 = csqr(Z8), Z32 = csqr(Z16), Z64 = csqr(Z32);
  {
    cpx2 C[8];
    C[0].re = (f32x2){1.f, 1.f}; C[0].im = (f32x2){0.f, 0.f};
    C[1] = Z8; C[2] = Z16; C[3] = cmul(Z8, Z16); C[4] = Z32; C[5] = cmul(Z32, Z8); C[6] = cmul(Z32, Z16); C[7] = cmul(Z32, C[3]);
#pragma unroll
    for (int j = 0; j < 8; ++j) put_rowc(stg, j, C[j]);
    put_rowc(stg, 8, Z64);
#pragma unroll
    for (int j = 1; j < 8; ++j) put_rowc(stg, 8 + j, cmul(Z64, C[j]));
  }
  u32x4 Bf[8];
#pragma unroll
  for (int ks = 0; ks < 8; ++ks) Bf[ks] = *(LAS u32x4*)(frd + 64 * ks);
  const cpx2 Z1 = cexp_m(al, th, 1.f);
  const cpx2 Z2 = csqr(Z1), Z4 = csqr(Z2);
  cpx2 Sm[8];
  Sm[1] = Z1; Sm[2] = Z2; Sm[3] = cmul(Z1, Z2); Sm[4] = Z4; Sm[5] = cmul(Z4, Z1); Sm[6] = cmul(Z4, Z2); Sm[7] = cmul(Z4, Sm[3]);
  const cpx2 Z128 = cexp_m(al, th, 128.f);
  const cpx2 Z256 = csqr(Z128), Z512 = csqr(Z256);
  const cpx2 Z1024 = cexp_m(al, th, 1024.f);
  const cpx2 Z2048 = csqr(Z1024);
  f32x4 kacc[3];
  {
    const cpx2 B1 = cmul(W, Z128);
    put_row(stg, 0, W);
#pragma unroll
    for (int i = 1; i < 8; ++i) put_row(stg, i, cmul(W, Sm[i]));
    put_row(stg, 8, B1);
#pragma unroll
    for (int i = 1; i < 8; ++i) put_row(stg, 8 + i, cmul(B1, Sm[i]));
    f32x4 acc = {0, 0, 0, 0};
#pragma unroll
    for (int ks = 0; ks < 8; ++ks) acc = mfma_h(*(LAS u32x4*)(frd + 64 * ks), Bf[ks], acc);
    kacc[0] = acc;
  }
  const cpx2 Z384 = cmul(Z256, Z128);
  {
    const cpx2 B2 = cmul(W, Z256), B3 = cmul(W, Z384), B4 = cmul(W, Z512);
    const cpx2 B5 = cmul(B4, Z128), B6 = cmul(B4, Z256), B7 = cmul(B4, Z384);
    put_row(stg, 0, B2); put_row(stg, 1, cmul(B2, Sm[2])); put_row(stg, 2, cmul(B2, Sm[4])); put_row(stg, 3, cmul(B2, Sm[6]));
    put_row(stg, 4, B3); put_row(stg, 5, cmul(B3, Sm[2])); put_row(stg, 6, cmul(B3, Sm[4])); put_row(stg, 7, cmul(B3, Sm[6]));
    put_row(stg, 8, B4); put_row(stg, 9, cmul(B4, Sm[4])); put_row(stg, 10, B5); put_row(stg, 11, cmul(B5, Sm[4]));
    put_row(stg, 12, B6); put_row(stg, 13, cmul(B6, Sm[4])); put_row(stg, 14, B7); put_row(stg, 15, cmul(B7, Sm[4]));
    f32x4 acc = {0, 0, 0, 0};
#pragma unroll
    for (int ks = 0; ks < 8; ++ks) acc = mfma_h(*(LAS u32x4*)(frd + 64 * ks), Bf[ks], acc);
    kacc[1] = acc;
  }
  u32x4 uv[8];
  const u32 uoff = WS_XT + (u32)d * 8192;
#pragma unroll
  for (int i = 0; i < 8; ++i) ald16s(uv[i], rsws, (u32)lane * 16, uoff + i * 1024);
  {
    const cpx2 B8 = cmul(W, Z1024);
    const cpx2 Z640 = cmul(Z512, Z128), Z768 = cmul(Z512, Z256), Z896 = cmul(Z512, Z384);
    put_row(stg, 0, B8); put_row(stg, 1, cmul(B8, Z128)); put_row(stg, 2, cmul(B8, Z256)); put_row(stg, 3, cmul(B8, Z384));
    put_row(stg, 4, cmul(B8, Z512)); put_row(stg, 5, cmul(B8, Z640)); put_row(stg, 6, cmul(B8, Z768)); put_row(stg, 7, cmul(B8, Z896));
    put_row(stg, 8, cmul(W, Z2048));
    f32x4 acc = {0, 0, 0, 0};
#pragma unroll
    for (int ks = 0; ks < 8; ++ks) acc = mfma_h(*(LAS u32x4*)(frd + 64 * ks), Bf[ks], acc);
    kacc[2] = acc;
  }
  {
    LAS float* kt = (LAS float*)(lds + LB_KT) + (ch * 2 + half) * 768;
    const float isr = rcpf_(SR);
#pragma unroll
    for (int t = 0; t < 3; ++t)
#pragma unroll
      for (int r = 0; r < 4; ++r) kt[(16 * t + 4 * g + r) * 16 + l16] = kacc[t][r] * isr;
    if (lane == 0) misc[w] = wsum;
  }
  TS(3);
  bar();
  const float SK0 = pow2_scale(misc[0] + misc[1], 14), SK1 = pow2_scale(misc[2] + misc[3], 14);
  {
    LAS float* kt = (LAS float*)(lds + LB_KT);
    const int i = tid >> 4, j = tid & 15;
    const int p0 = (i & 7) + 8 * j + 128 * (i >> 3);
    const int p1 = w < 2 ? 256 + (i & 3) + 4 * j + 64 * (i >> 2) : 384 + (i & 1) + 2 * j + 32 * ((i - 8) >> 1);
    const int p2 = w < 2 ? 512 + j + 16 * i : (tid == 128 ? 640 : 642);
    const int pos[3] = {p0, p1, p2};
#pragma unroll
    for (int c = 0; c < 2; ++c)
#pragma unroll
      for (int t = 0; t < 3; ++t) {
        const int idx = tid + 256 * t;
        const float v = (kt[(c * 2) * 768 + idx] + kt[(c * 2 + 1) * 768 + idx]) * (c ? SK1 : SK0);
        *(LAS _Float16*)(lds + LB_KC + c * 1536 + pos[t] * 2) = (_Float16)v;
      }
    if (tid < 2) *(LAS u16*)(lds + LB_KC + tid * 1536 + 641 * 2) = 0;
  }
  bar();
  TS(4);
#pragma unroll
  for (int c = 0; c < 2; ++c) {
    u32 dw[6];
#pragma unroll
    for (int k = 0; k < 5; ++k) {
      u16x2 v;
      v.x = *(LAS u16*)(lds + c * 1536 + ko[2 * k]);
      v.y = *(LAS u16*)(lds + c * 1536 + ko[2 * k + 1]);
      dw[k] = __builtin_bit_cast(u32, v);
    }
    dw[5] = *(LAS u16*)(lds + c * 1536 + ko[10]);
    u32 od[5];
#pragma unroll
    for (int k = 0; k < 5; ++k) od[k] = __builtin_amdgcn_alignbit(dw[k + 1], dw[k], 16);
    LAS char* cp = lds + LB_CP + c * CPCH + tid * 16;
    *(LAS u32x4*)(cp) = (u32x4){dw[0], dw[1], dw[2], dw[3]};
    *(LAS u32x4*)(cp + CPS) = (u32x4){od[0], od[1], od[2], od[3]};
    *(LAS u32x4*)(cp + 2 * CPS) = (u32x4){dw[1], dw[2], dw[3], dw[4]};
    *(LAS u32x4*)(cp + 3 * CPS) = (u32x4){od[1], od[2], od[3], od[4]};
  }
  if (tid < 32) *(LAS u32x4*)(lds + LB_CP + (tid >> 2) * CPS + 4096 + (tid & 3) * 16) = (u32x4){0, 0, 0, 0};
  TS(5);
  {
    waitv4<0>(uv[0], uv[1], uv[2], uv[3]);
#pragma unroll
    for (int i = 4; i < 8; ++i) tie(uv[i]);
    u32 spins = 0;
#pragma unroll 1
    for (;;) {
      bool ok = true;
#pragma unroll
      for (int i = 0; i < 8; ++i) ok &= (uv[i][1] == tag) & (uv[i][3] == tag);
      if (__all(ok) || spin_fail(spins, rsws)) break;
#pragma unroll
      for (int i = 0; i < 8; ++i) uv[i] = ld16_sc1(rsws, uoff + i * 1024 + (u32)lane * 16);
    }
#pragma unroll
    for (int i = 0; i < 4; ++i) tie(pf[i]);
    u32 m = 0;
#pragma unroll
    for (int i = 0; i < 8; ++i) {
      const u32 a = uv[i][0] & 0x7fff7fffu, b = uv[i][2] & 0x7fff7fffu;
      m = max(m, max(max(a << 16, a & 0xffff0000u), max(b << 16, b & 0xffff0000u)));
    }
    const float SU = pow2_scale(wave_max(u2f(m)), 14);
    LAS char* ub = lds + LB_U + ch * 4608;
    const int sg = lane & 15;
    const int pi4 = 8 * (sg >> 3) + 2 * (sg & 3) + ((sg >> 2) & 1);
    LAS char* uw = ub + 512 + (64 * (lane >> 4) + 4 * pi4) * 2;
    if (half) {
#pragma unroll
      for (int i = 4; i < 8; ++i) { const u32 a = uv[i][0], b = uv[i][2]; *(LAS u32x2*)(uw + i * 512) = (u32x2){pk_f16(bf2f(a) * SU, bfhi(a) * SU), pk_f16(bf2f(b) * SU, bfhi(b) * SU)}; }
      asm volatile("" ::: "memory");
    } else {
#pragma unroll
      for (int i = 0; i < 4; ++i) { const u32 a = uv[i][0], b = uv[i][2]; *(LAS u32x2*)(uw + i * 512) = (u32x2){pk_f16(bf2f(a) * SU, bfhi(a) * SU), pk_f16(bf2f(b) * SU, bfhi(b) * SU)}; }
    }
    if (half == 0) { if (lane < 32) *(LAS u32x4*)(ub + lane * 16) = (u32x4){0, 0, 0, 0}; if (lane == 0) misc[4 + ch] = SU; }
  }
  TS(6);
  bar();
  TS(7);
  f32x4 acc[8];
#pragma unroll
  for (int i = 0; i < 8; ++i) acc[i] = (f32x4){0, 0, 0, 0};
  {
    const int cs = (3 - l16) & 3;
    LAS char* abase = lds + LB_CP + ch * CPCH + cs * CPS + (2031 - l16 + 8 * g - cs) * 2 - 512 * 7 - 64 * (4 * half + 3);
    LAS char* bbase = lds + LB_U + ch * 4608 + (256 + 16 * (l16 - 1) + 8 * g) * 2 - 64 * (4 * half + 3);
    LAS char* abase2 = abase + 8;
    asm volatile("" : "+v"(abase2));
    u32x4 A[2][8], B[2][8];
    auto ldfr = [&](int it, int buf) {
#pragma unroll
      for (int s = 0; s < 8; ++s) {
        const int off = 512 * (7 - s) + 64 * (3 - it);
        A[buf][s] = cat(*(LAS u32x2*)(abase + off), *(LAS u32x2*)(abase2 + off));
        B[buf][s] = *(LAS u32x4*)(bbase + 512 * s + 64 * (3 - it));
      }
    };
    ldfr(0, 0);
#pragma unroll
    for (int it = 0; it < 4; ++it) {
      if (it < 3) ldfr(it + 1, (it + 1) & 1);
#pragma unroll
      for (int cb = 0; cb < 8; ++cb)
#pragma unroll
        for (int s = 0; s <= cb; ++s) acc[cb] = mfma_h(A[it & 1][s], B[it & 1][cb - s], acc[cb]);
      __builtin_amdgcn_sched_barrier(0);
    }
  }
  TS(8);
  {
    LAS char* ex = lds + LB_EX;
    f32x4 own0 = acc[0], own1 = acc[1];
#pragma unroll
    for (int pw = 0; pw < 4; ++pw) {
      if (pw != w) {
        LAS char* dst = ex + ((pw * 3 + (w > pw ? w - 1 : w)) * 2) * 1024 + lane * 16;
        *(LAS f32x4*)(dst) = acc[2 * pw]; *(LAS f32x4*)(dst + 1024) = acc[2 * pw + 1];
      } else { own0 = acc[2 * pw]; own1 = acc[2 * pw + 1]; }
    }
    bar();
    f32x4 v0[4], v1[4];
#pragma unroll
    for (int sw = 0; sw < 4; ++sw) {
      if (sw != w) {
        LAS char* src = ex + ((w * 3 + (sw > w ? sw - 1 : sw)) * 2) * 1024 + lane * 16;
        v0[sw] = *(LAS f32x4*)(src); v1[sw] = *(LAS f32x4*)(src + 1024);
      } else { v0[sw] = own0; v1[sw] = own1; }
    }
    const float SU0 = misc[4], SU1 = misc[5];
    const float inv0 = rcpf_(SK0 * SU0), inv1 = rcpf_(SK1 * SU1);
    const float sd0 = rcpf_(SU0), sd1 = rcpf_(SU1);
    const float dk0 = bf2f(dsk2), dk1 = bfhi(dsk2);
#pragma unroll
    for (int ci = 0; ci < 2; ++ci) {
      const int cb = 2 * w + ci;
      const f32x4 y0 = ci ? v1[0] + v1[1] : v0[0] + v0[1];
      const f32x4 y1 = ci ? v1[2] + v1[3] : v0[2] + v0[3];
      const int l0 = 256 * cb + 16 * l16 + 4 * g;
      const f16x4 u0 = *(LAS f16x4*)(lds + LB_U + 512 + l0 * 2);
      const f16x4 u1 = *(LAS f16x4*)(lds + LB_U + 4608 + 512 + l0 * 2);
      u32 P[4];
#pragma unroll
      for (int r = 0; r < 4; ++r) {
        const float b0 = rbf(y0[r] * inv0) + rbf((float)u0[r] * sd0 * dk0);
        const float b1 = rbf(y1[r] * inv1) + rbf((float)u1[r] * sd1 * dk1);
        P[r] = pk_bf16(b0, b1);
      }
      const u32 a0 = WS_YB + (u32)pair * 16384 + (u32)cb * 2048 + (u32)l16 * 64 + (u32)g * 16;
      st16_sc1(rsws, a0, (u32x4){P[0], tag, P[1], tag});
      st16_sc1(rsws, a0 + 1024, (u32x4){P[2], tag, P[3], tag});
    }
  }
  TS(9);
  TS_OUT();
}

DI void roleG(const Params& p, int xcd, int o, u32 ev, rsrc_t rsws) {
  const int tid = threadIdx.x;
  const int gb = 8 * (xcd >> 1) + 4 * (xcd & 1) + (o - 28);
  const int row = gb * 64 + (tid >> 2), part = tid & 3;
  init_sentinels(xcd, o, rsws);
  float s = 0.f;
#pragma unroll
  for (int i = 0; i < 14; ++i) {
    const u32x4 xv = *(const u32x4*)(p.x + (size_t)row * D + (i * 4 + part) * 8);
    const u32x4 gv = *(const u32x4*)(p.gatew + (i * 4 + part) * 8);
#pragma unroll
    for (int k = 0; k < 4; ++k) {
      const u32 r = pk_bf16(bf2f(xv[k]) * bf2f(gv[k]), bfhi(xv[k]) * bfhi(gv[k]));
      s += bf2f(r); s += bfhi(r);
    }
  }
  asm volatile("s_waitcnt vmcnt(0)" : "+v"(ev) :: "memory");
  const u32 tag = (u32)__builtin_amdgcn_readfirstlane(ev) + 1u;
  bar();
  if (tid == 0) st4_sc1(rsws, WS_SEEN + (u32)(o * 8 + xcd) * 4, tag);
  s += dpp<0xB1>(s); s += dpp<0x4E>(s);
  if (part == 0) { const float lg = rbf(s); st8_sc1(rsws, WS_G + (u32)row * 8, (u32x2){f2u(rbf(rcpf_(1.f + exp2f_(-lg * LOG2E)))), tag}); }
  if (gb == 0 && tid < 64) {
    u32 spins = 0;
#pragma unroll 1
    for (;;) {
      const u32x4 a = ld16_sc1(rsws, WS_SEEN + (u32)tid * 16), b = ld16_sc1(rsws, WS_SEEN + 1024 + (u32)tid * 16);
      const bool ok = (a[0] == tag) & (a[1] == tag) & (a[2] == tag) & (a[3] == tag) & (b[0] == tag) & (b[1] == tag) & (b[2] == tag) & (b[3] == tag);
      if (__all(ok) || spin_fail(spins, rsws)) break;
      __builtin_amdgcn_s_sleep(8);
    }
    if (tid == 0) st4_sc1(rsws, WS_EPOCH, tag);
  }
}

__global__ void __launch_bounds__(256, 2) k_main(Params p) {
  extern __shared__ __attribute__((aligned(16))) char smem[];
  LAS char* lds = (LAS char*)smem;
  const rsrc_t rsws = mkrs(p.ws, WS_USED);
  const int b = blockIdx.x, xcd = b & 7, j = b >> 3;
  const u32 ev = ld4_sc1(rsws, WS_EPOCH);
  asm volatile("" ::: "memory");
  const int o = j & 31;
  if (j < 32) { roleA(p, xcd, o, lds, ev, rsws); }
  else if (o < 28) { roleB(p, xcd, o, lds, ev, rsws); }
  else { roleG(p, xcd, o, ev, rsws); }
}

extern "C" void kernel_launch(void* const* d_in, const int* in_sizes, int n_in, void* d_out, int out_size, void* d_ws, size_t ws_size, hipStream_t stream) {
  Params p;
  p.x = (const u16*)d_in[0]; p.wqkv = (const u16*)d_in[1]; p.wphi = (const u16*)d_in[2]; p.wo = (const u16*)d_in[3];
  p.Are = (const u16*)d_in[5]; p.Aim = (const u16*)d_in[6]; p.Bre = (const u16*)d_in[7]; p.Bim = (const u16*)d_in[8];
  p.Cre = (const u16*)d_in[9]; p.Cim = (const u16*)d_in[10]; p.logdt = (const u16*)d_in[11]; p.dskip = (const u16*)d_in[12]; p.gatew = (const u16*)d_in[13];
  p.out = (u16*)d_out; p.ws = (char*)d_ws;
  (void)hipFuncSetAttribute((const void*)k_main, hipFuncAttributeMaxDynamicSharedMemorySize, LDS_BYTES);
  k_main<<<512, 256, LDS_BYTES, stream>>>(p);
}
```

```cpp
#include <hip/hip_runtime.h>
#include <stdint.h>

typedef unsigned short u16;
typedef unsigned int u32;
typedef unsigned long long u64;
typedef short s16x4 __attribute__((ext_vector_type(4)));
typedef _Float16 f16x8 __attribute__((ext_vector_type(8)));
typedef _Float16 f16x2 __attribute__((ext_vector_type(2)));
typedef _Float16 f16x4 __attribute__((ext_vector_type(4)));
typedef __bf16 bf16x8 __attribute__((ext_vector_type(8)));
typedef __bf16 bf16x2 __attribute__((ext_vector_type(2)));
typedef float f32x16 __attribute__((ext_vector_type(16)));
typedef float f32x4 __attribute__((ext_vector_type(4)));
typedef float f32x2 __attribute__((ext_vector_type(2)));
typedef u32 u32x4 __attribute__((ext_vector_type(4)));
typedef u32 u32x2 __attribute__((ext_vector_type(2)));
typedef u16 u16x2 __attribute__((ext_vector_type(2)));
#define SETPRIO(x) __builtin_amdgcn_s_setprio(x)
#define LAS __attribute__((address_space(3)))
#define DI __device__ __forceinline__
typedef __amdgpu_buffer_rsrc_t rsrc_t;

constexpr int S = 2048, D = 448, H = 16, DH = 28, NS = 256;
constexpr float LOG2E = 1.4426950408889634f;
constexpr float INV2PI = 0.15915494309189535f;

constexpr u32 WS_EPOCH = 0;
constexpr u32 WS_TMO   = 256;
constexpr u32 WS_SEEN  = 4096;
constexpr u32 WS_G     = 16384;
constexpr u32 WS_G2    = 64u << 10;
constexpr u32 WS_G1    = 1u << 20;
constexpr u32 WS_OF    = 3u << 20;
constexpr u32 WS_XT    = 5u << 20;
constexpr u32 WS_YB    = 9u << 20;
constexpr u32 WS_ST    = 15u << 20;
constexpr u32 WS_USED  = 16u << 20;
constexpr u32 XTS = 449 * 256, YBS = 8192 + 128;
constexpr u32 SENT = 0xFFFFFFFFu;
constexpr u32 OOB = 0x40000000u;

#define TSX(i)
#define TS_DECL
#define TS(i)
#define TS_OUT()

struct Params {
  const u16 *x, *wqkv, *wphi, *wo, *Are, *Aim, *Bre, *Bim, *Cre, *Cim, *logdt, *dskip, *gatew;
  u16* out; char* ws;
};

DI u32 f2u(float v) { return __builtin_bit_cast(u32, v); }
DI float u2f(u32 v) { return __builtin_bit_cast(float, v); }
DI float bf2f(u32 v) { return __builtin_bit_cast(float, v << 16); }
DI float bfhi(u32 v) { return __builtin_bit_cast(float, v & 0xffff0000u); }
DI u32 pk_bf16(float a, float b) { f32x2 v = {a, b}; return __builtin_bit_cast(u32, __builtin_convertvector(v, bf16x2)); }
DI u32 pk_f16(float a, float b) { f32x2 v = {a, b}; return __builtin_bit_cast(u32, __builtin_convertvector(v, f16x2)); }
DI float rbf(float a) { return u2f(pk_bf16(0.f, a)); }
DI f32x4 mfma16(u32x4 a, u32x4 b, f32x4 c) { return __builtin_amdgcn_mfma_f32_16x16x32_bf16(__builtin_bit_cast(bf16x8, a), __builtin_bit_cast(bf16x8, b), c, 0, 0, 0); }
DI f32x16 mfma32(u32x4 a, u32x4 b, f32x16 c) { return __builtin_amdgcn_mfma_f32_32x32x16_bf16(__builtin_bit_cast(bf16x8, a), __builtin_bit_cast(bf16x8, b), c, 0, 0, 0); }
DI f32x4 mfma_h(u32x4 a, u32x4 b, f32x4 c) { return __builtin_amdgcn_mfma_f32_16x16x32_f16(__builtin_bit_cast(f16x8, a), __builtin_bit_cast(f16x8, b), c, 0, 0, 0); }
DI u32x2 tr_read(LAS char* p) { return __builtin_bit_cast(u32x2, __builtin_amdgcn_ds_read_tr16_b64_v4i16((LAS s16x4*)p)); }
DI u32x4 cat(u32x2 a, u32x2 b) { return (u32x4){a.x, a.y, b.x, b.y}; }
template <int CTRL> DI float dpp(float x) { return __builtin_bit_cast(float, __builtin_amdgcn_mov_dpp(__builtin_bit_cast(int, x), CTRL, 0xf, 0xf, true)); }
DI float row16_max(float x) { x = fmaxf(x, dpp<0xB1>(x)); x = fmaxf(x, dpp<0x4E>(x)); x = fmaxf(x, dpp<0x141>(x)); x = fmaxf(x, dpp<0x140>(x)); return x; }
DI float row16_sum(float x) { x += dpp<0xB1>(x); x += dpp<0x4E>(x); x += dpp<0x141>(x); x += dpp<0x140>(x); return x; }
DI void swap16(float x, float& a, float& b) { const int u = __builtin_bit_cast(int, x); auto r = __builtin_amdgcn_permlane16_swap(u, u, false, false); const int r0 = r[0], r1 = r[1]; a = __builtin_bit_cast(float, r0); b = __builtin_bit_cast(float, r1); }
DI void swap32(float x, float& a, float& b) { const int u = __builtin_bit_cast(int, x); auto r = __builtin_amdgcn_permlane32_swap(u, u, false, false); const int r0 = r[0], r1 = r[1]; a = __builtin_bit_cast(float, r0); b = __builtin_bit_cast(float, r1); }
DI float wave_max(float x) { float a, b; x = row16_max(x); swap16(x, a, b); x = fmaxf(a, b); swap32(x, a, b); return fmaxf(a, b); }
DI float wave_sum(float x) { float a, b; x = row16_sum(x); swap16(x, a, b); x = a + b; swap32(x, a, b); return a + b; }
DI float exp2f_(float x) { return __builtin_amdgcn_exp2f(x); }
DI float rcpf_(float x) { return __builtin_amdgcn_rcpf(x); }
DI rsrc_t mkrs(const void* p, u32 bytes) { return __builtin_amdgcn_make_buffer_rsrc((void*)p, 0, (int)bytes, 0x00020000); }
DI void dma16(u32 lds_addr, u32 voff, rsrc_t rs, u32 soff) {
  asm volatile("s_mov_b32 m0, %0\n\ts_nop 0\n\tbuffer_load_dwordx4 %1, %2, %3 offen lds" :: "s"(lds_addr), "v"(voff), "s"(rs), "s"(soff) : "memory");
}
DI void dma16n(u32 lds_addr, u32 voff, rsrc_t rs, u32 soff) {
  asm volatile("s_mov_b32 m0, %0\n\ts_nop 0\n\tbuffer_load_dwordx4 %1, %2, %3 offen lds" :: "s"(lds_addr), "v"(voff), "s"(rs), "s"(soff));
}
template <int N> DI void waitvm() { asm volatile("s_waitcnt vmcnt(%0)" :: "n"(N) : "memory"); }
DI void bar() { asm volatile("s_waitcnt lgkmcnt(0)" ::: "memory"); __builtin_amdgcn_s_barrier(); asm volatile("" ::: "memory"); }
constexpr int AUX_SC1 = 16;
DI u32x4 ld16_sc1(rsrc_t rs, u32 off) { return __builtin_amdgcn_raw_buffer_load_b128(rs, off, 0, AUX_SC1); }
DI u32x2 ld8_sc1(rsrc_t rs, u32 off) { return __builtin_amdgcn_raw_buffer_load_b64(rs, off, 0, AUX_SC1); }
DI u32 ld4_sc1(rsrc_t rs, u32 off) { return __builtin_amdgcn_raw_buffer_load_b32(rs, off, 0, AUX_SC1); }
DI void st16_sc1(rsrc_t rs, u32 off, u32x4 v) { __builtin_amdgcn_raw_buffer_store_b128(v, rs, off, 0, AUX_SC1); }
DI void st8_sc1(rsrc_t rs, u32 off, u32x2 v) { __builtin_amdgcn_raw_buffer_store_b64(v, rs, off, 0, AUX_SC1); }
DI void st4_sc1(rsrc_t rs, u32 off, u32 v) { __builtin_amdgcn_raw_buffer_store_b32(v, rs, off, 0, AUX_SC1); }
DI void ald16(u32x4& d, rsrc_t rs, u32 voff, u32 soff) { asm volatile("buffer_load_dwordx4 %0, %1, %2, %3 offen" : "=v"(d) : "v"(voff), "s"(rs), "s"(soff) : "memory"); }
DI void ald16s(u32x4& d, rsrc_t rs, u32 voff, u32 soff) { asm volatile("buffer_load_dwordx4 %0, %1, %2, %3 offen sc1" : "=v"(d) : "v"(voff), "s"(rs), "s"(soff) : "memory"); }
DI void ald8s(u32x2& d, rsrc_t rs, u32 voff, u32 soff) { asm volatile("buffer_load_dwordx2 %0, %1, %2, %3 offen sc1" : "=v"(d) : "v"(voff), "s"(rs), "s"(soff) : "memory"); }
template <int N> DI void waitv(u32x4& a) { asm volatile("s_waitcnt vmcnt(%1)" : "+v"(a) : "n"(N) : "memory"); }
template <int N> DI void waitv4(u32x4& a, u32x4& b, u32x4& c, u32x4& d) { asm volatile("s_waitcnt vmcnt(%4)" : "+v"(a), "+v"(b), "+v"(c), "+v"(d) : "n"(N) : "memory"); }
DI void tie(u32x4& a) { asm volatile("" : "+v"(a) :: "memory"); }
DI u32 max4(u32x4 v) { return max(max(v[0], v[1]), max(v[2], v[3])); }
DI u32 sane(u32 v) { return min(v, 0xFFFFFFFEu); }
constexpr u32 SPIN_CAP = 1u << 14;
DI bool spin_fail(u32& spins, rsrc_t rsws) {
  __builtin_amdgcn_s_sleep(2);
  asm volatile("" ::: "memory");
  if (++spins > SPIN_CAP) { st4_sc1(rsws, WS_TMO, 1u); return true; }
  return false;
}

constexpr int LDS_BYTES = 75776;

constexpr int LA_WK = 0, LA_WV = 28672, LA_XS = 57344, LA_WP = 73728;
constexpr int LA_WQ = LA_WK, LA_V = LA_WV, LA_WO = 0;
#define LOOKA 3
struct ASeq { int w[7]; };
constexpr ASeq make_aseq() {
  ASeq r{};
  int seq[128] = {}; int n = 0;
  for (int k = 0; k < LOOKA && k < 7; ++k) for (int e = 0; e < 6; ++e) seq[n++] = k;
  for (int c = 0; c < 7; ++c) {
    int last = -1;
    for (int i = 0; i < n; ++i) if (seq[i] == c) last = i;
    r.w[c] = n - 1 - last;
    if (c + LOOKA < 7) for (int e = 0; e < 6; ++e) seq[n++] = c + LOOKA;
    if (c == 0) { seq[n++] = -1; }
  }
  r.w[6] = 0;
  return r;
}
constexpr ASeq ASEQ = make_aseq();

DI void softmax32(const f32x16& pin, bool hi, u32x4 (&fp)[2], u32x4 (&fn)[2]) {
  float t[16];
#pragma unroll
  for (int i = 0; i < 16; ++i) t[i] = rbf(pin[i]);
  float tp[4], tn[4];
#pragma unroll
  for (int i = 0; i < 4; ++i) { tp[i] = hi ? -__builtin_inff() : t[12 + i]; tn[i] = hi ? __builtin_inff() : t[12 + i]; }
  float mx = fmaxf(fmaxf(t[0], t[1]), t[2]), mn = fminf(fminf(t[0], t[1]), t[2]);
#pragma unroll
  for (int i = 3; i < 11; i += 2) { mx = fmaxf(fmaxf(mx, t[i]), t[i + 1]); mn = fminf(fminf(mn, t[i]), t[i + 1]); }
  mx = fmaxf(fmaxf(mx, t[11]), tp[0]); mx = fmaxf(fmaxf(mx, tp[1]), tp[2]); mx = fmaxf(mx, tp[3]);
  mn = fminf(fminf(mn, t[11]), tn[0]); mn = fminf(fminf(mn, tn[1]), tn[2]); mn = fminf(mn, tn[3]);
  { float a, b; swap32(mx, a, b); mx = fmaxf(a, b); swap32(mn, a, b); mn = fminf(a, b); }
  const f32x2 L2 = {LOG2E, LOG2E}, NL2 = {-LOG2E, -LOG2E};
  const f32x2 mxs = (f32x2){mx, mx} * NL2, mns = (f32x2){mn, mn} * L2;
  float ep[16], en[16];
#pragma unroll
  for (int i = 0; i < 16; i += 2) {
    const f32x2 a = i < 12 ? (f32x2){t[i], t[i + 1]} : (f32x2){tp[i - 12], tp[i - 11]};
    const f32x2 b = i < 12 ? (f32x2){t[i], t[i + 1]} : (f32x2){tn[i - 12], tn[i - 11]};
    const f32x2 xp = __builtin_elementwise_fma(a, L2, mxs), xn = __builtin_elementwise_fma(b, NL2, mns);
    ep[i] = exp2f_(xp[0]); ep[i + 1] = exp2f_(xp[1]); en[i] = exp2f_(xn[0]); en[i + 1] = exp2f_(xn[1]);
  }
  f32x2 sp2 = {ep[0], ep[1]}, sn2 = {en[0], en[1]};
#pragma unroll
  for (int i = 2; i < 16; i += 2) { sp2 += (f32x2){ep[i], ep[i + 1]}; sn2 += (f32x2){en[i], en[i + 1]}; }
  float sp = sp2[0] + sp2[1], sn = sn2[0] + sn2[1];
  { float a, b; swap32(sp, a, b); sp = a + b; swap32(sn, a, b); sn = a + b; }
  const float ip = rcpf_(sp), in = rcpf_(sn);
#pragma unroll
  for (int s = 0; s < 2; ++s)
#pragma unroll
    for (int k = 0; k < 4; ++k) {
      const f32x2 a = (f32x2){ep[8 * s + 2 * k], ep[8 * s + 2 * k + 1]} * (f32x2){ip, ip};
      const f32x2 b = (f32x2){en[8 * s + 2 * k], en[8 * s + 2 * k + 1]} * (f32x2){in, in};
      fp[s][k] = pk_bf16(a[0], a[1]);
      fn[s][k] = pk_bf16(b[0], b[1]);
    }
}
DI u32x4 pack8(const f32x16& a, int s) { return (u32x4){pk_bf16(a[8 * s], a[8 * s + 1]), pk_bf16(a[8 * s + 2], a[8 * s + 3]), pk_bf16(a[8 * s + 4], a[8 * s + 5]), pk_bf16(a[8 * s + 6], a[8 * s + 7])}; }

DI void roleA(const Params& p, int xcd, int o, LAS char* lds, u32 ev, rsrc_t rsws) {
  TS_DECL;
  TS(0);
  const int tid = threadIdx.x, lane = tid & 63, w = __builtin_amdgcn_readfirstlane(tid >> 6);
  const int h = 8 * (xcd & 1) + (int)((((xcd & 1) ? 0x64207531u : 0x75310642u) >> (4 * (o & 7))) & 7u), rr = 4 * (xcd >> 1) + (o >> 3);
  const int wsh = h == 15 ? 8 : 0;
  const int l32 = lane & 31, hh = lane >> 5, l16 = lane & 15, g = lane >> 4;
  const bool hi = lane >= 32;
  SETPRIO(2);
  const u32 ldsb = (u32)(uintptr_t)lds;
  const rsrc_t rsW = mkrs(p.wqkv, D * 3 * D * 2), rsP = mkrs(p.wphi, H * DH * DH * 2), rsX = mkrs(p.x, S * D * 2);
  const u32 wvoff = ((lane >> 2) * (3 * D) + h * DH) * 2 + (lane & 3) * 16 - wsh;
  const int r0 = rr * 128 + w * 32;
  int c0 = 2 * (rr & 3) + (h & 7); c0 = c0 >= 7 ? c0 - 7 : c0; c0 = c0 >= 7 ? c0 - 7 : c0;
  u32x4 xr[7][4];
  const u32 xvoff = (r0 + (lane >> 3)) * (D * 2) + 16 * (lane & 7);
  const u32 wv2 = wvoff + (1 + (w >> 1)) * (D * 2);
  const u32 lw = ldsb + LA_WK + (w >> 1) * 28672 + (w & 1) * 2048;
  const u32 so = (w & 1) * 32 * (3 * D * 2);
  auto chunk_of = [&](int i) { int c = c0 + i; return c >= 7 ? c - 7 : c; };
  auto issue = [&](int i) {
    const u32 c = chunk_of(i);
    dma16(lw + i * 4096, wv2, rsW, so + c * 64 * (3 * D * 2));
    dma16(lw + i * 4096 + 1024, wv2, rsW, so + c * 64 * (3 * D * 2) + 16 * (3 * D * 2));
#pragma unroll
    for (int k = 0; k < 4; ++k) ald16(xr[i][k], rsX, xvoff, c * 128 + k * 8 * D * 2);
  };
  dma16(ldsb + LA_WP + (w & 1) * 1024, (h * (DH * DH) + ((w & 1) * 16 + (lane >> 2)) * DH) * 2 + (lane & 3) * 16, rsP, 0);
  const u32 ob = WS_OF + (u32)(((r0 >> 5) * 16 + h) * 2) * 1024 + lane * 16;
  const u32 g2v = tid < 16 ? WS_G2 + (u32)h * 4096 + (u32)(rr >> 2) * 1024 + (u32)(((rr & 1) * 32 + ((rr >> 1) & 1) * 16 + tid) * 16) : OOB;
#pragma unroll
  for (int i = 0; i < LOOKA; ++i) issue(i);
  TS(1);
  LAS char* xs = lds + LA_XS + w * 4096;
  const int r3 = lane >> 3, ch8 = lane & 7;
  LAS char* wa0 = xs + r3 * 128 + ((ch8 ^ (r3 >> 1)) << 4);
  LAS char* wa1 = xs + r3 * 128 + ((ch8 ^ (r3 >> 1) ^ 4) << 4);
  LAS char* ra[4];
#pragma unroll
  for (int j = 0; j < 4; ++j) ra[j] = xs + l32 * 128 + (((2 * j + hh) ^ ((l32 >> 1) & 7)) << 4);
  LAS char* wb = lds + (8 * hh + ((lane & 15) >> 2)) * 64 + 32 * ((lane >> 4) & 1) + 8 * (lane & 3) + wsh;
  u32x4 xf[28];
  f32x16 ak, av;
#pragma unroll
  for (int i = 0; i < 16; ++i) { ak[i] = 0.f; av[i] = 0.f; }
  u32x4 wk[2][4], wv[2][4];
  u32 tag = 0;
#pragma unroll
  for (int c = 0; c < 8; ++c) {
    if (c < 7) {
      if (c == 0) waitv4<ASEQ.w[0]>(xr[0][0], xr[0][1], xr[0][2], xr[0][3]);
      else if (c == 1) waitv4<ASEQ.w[1]>(xr[1][0], xr[1][1], xr[1][2], xr[1][3]);
      else if (c == 2) waitv4<ASEQ.w[2]>(xr[2][0], xr[2][1], xr[2][2], xr[2][3]);
      else if (c == 3) waitv4<ASEQ.w[3]>(xr[3][0], xr[3][1], xr[3][2], xr[3][3]);
      else if (c == 4) waitv4<ASEQ.w[4]>(xr[4][0], xr[4][1], xr[4][2], xr[4][3]);
      else if (c == 5) waitv4<ASEQ.w[5]>(xr[5][0], xr[5][1], xr[5][2], xr[5][3]);
      else waitv4<ASEQ.w[6]>(xr[6][0], xr[6][1], xr[6][2], xr[6][3]);
      if (c == 6) { asm volatile("" : "+v"(ev)); tag = (u32)__builtin_amdgcn_readfirstlane(ev) + 1u; }
      __builtin_amdgcn_s_barrier();
      asm volatile("" ::: "memory");
      if (c == 0) TS(2);
      if (c == 3) TS(3);
      *(LAS u32x4*)(wa0) = xr[c][0]; *(LAS u32x4*)(wa1 + 1024) = xr[c][1]; *(LAS u32x4*)(wa0 + 2048) = xr[c][2]; *(LAS u32x4*)(wa1 + 3072) = xr[c][3];
#pragma unroll
      for (int j = 0; j < 4; ++j) {
        const int ks = 4 * c + j;
        xf[ks] = *(LAS u32x4*)(ra[j]);
        wk[c & 1][j] = cat(tr_read(wb + LA_WK + ks * 1024), tr_read(wb + LA_WK + ks * 1024 + 256));
        wv[c & 1][j] = cat(tr_read(wb + LA_WV + ks * 1024), tr_read(wb + LA_WV + ks * 1024 + 256));
      }
    }
    if (c > 0) {
#pragma unroll
      for (int j = 0; j < 4; ++j) {
        const int ks = 4 * (c - 1) + j;
        ak = mfma32(wk[(c - 1) & 1][j], xf[ks], ak);
        av = mfma32(wv[(c - 1) & 1][j], xf[ks], av);
      }
    }
    if (c < 7) {
#pragma unroll
      for (int j = 0; j < 8; ++j) { __builtin_amdgcn_sched_group_barrier(0x300, 3, 0); if (c > 0) __builtin_amdgcn_sched_group_barrier(0x8, 1, 0); }
    }
    __builtin_amdgcn_sched_barrier(0);
    if (c + LOOKA < 7) issue(c + LOOKA);
    if (c == 0) {
      st16_sc1(rsws, g2v, (u32x4){SENT, SENT, SENT, SENT});
    }
    if (c == 6) {
#pragma unroll
      for (int i = 0; i < 5; ++i) dma16(ldsb + LA_WQ + i * 4096 + w * 1024, wvoff, rsW, (chunk_of(i) * 64 + w * 16) * (3 * D * 2));
    }
  }
  TS(4);
  u32x4 wf[2];
  {
    LAS char* pb = lds + LA_WP + (4 * hh + ((lane & 15) >> 2)) * 64 + 32 * ((lane >> 4) & 1) + 8 * (lane & 3);
#pragma unroll
    for (int s = 0; s < 2; ++s) wf[s] = cat(tr_read(pb + s * 1024), tr_read(pb + s * 1024 + 512));
  }
  bar();
#pragma unroll
  for (int i = 5; i < 7; ++i) dma16(ldsb + LA_WQ + i * 4096 + w * 1024, wvoff, rsW, (chunk_of(i) * 64 + w * 16) * (3 * D * 2));
  st16_sc1(rsws, ob, (u32x4){SENT, SENT, SENT, SENT}); st16_sc1(rsws, ob + 1024, (u32x4){SENT, SENT, SENT, SENT});
  f32x16 z16;
#pragma unroll
  for (int i = 0; i < 16; ++i) z16[i] = 0.f;
  {
    u32x4 k0 = pack8(ak, 0), k1 = pack8(ak, 1);
    if (hi) { k1[2] = 0; k1[3] = 0; }
    f32x16 pk = mfma32(wf[0], k0, z16);
    pk = mfma32(wf[1], k1, pk);
    const int sw = (l32 >> 1) & 7;
    LAS char* vt = lds + LA_V + w * 2048 + l32 * 64;
    {
      u32x4 v0 = pack8(av, 0), v1 = pack8(av, 1);
      if (hi) { v1[2] = 0x00003f80u; v1[3] = 0; }
      *(LAS u32x2*)(vt + 8 * ((hh) ^ sw)) = (u32x2){v0[0], v0[1]};
      *(LAS u32x2*)(vt + 8 * ((2 + hh) ^ sw)) = (u32x2){v0[2], v0[3]};
      *(LAS u32x2*)(vt + 8 * ((4 + hh) ^ sw)) = (u32x2){v1[0], v1[1]};
      *(LAS u32x2*)(vt + 8 * ((6 + hh) ^ sw)) = (u32x2){v1[2], v1[3]};
    }
    u32x4 fp[2], fn[2];
    softmax32(pk, hi, fp, fn);
#pragma unroll
    for (int s = 0; s < 2; ++s) {
      *(LAS u32x2*)(xs + l32 * 64 + 8 * ((4 * s + hh) ^ sw)) = (u32x2){fp[s][0], fp[s][1]};
      *(LAS u32x2*)(xs + l32 * 64 + 8 * ((4 * s + 2 + hh) ^ sw)) = (u32x2){fp[s][2], fp[s][3]};
      *(LAS u32x2*)(xs + 2048 + l32 * 64 + 8 * ((4 * s + hh) ^ sw)) = (u32x2){fn[s][0], fn[s][1]};
      *(LAS u32x2*)(xs + 2048 + l32 * 64 + 8 * ((4 * s + 2 + hh) ^ sw)) = (u32x2){fn[s][2], fn[s][3]};
    }
  }
  TS(5);
  bar();
  {
    const int q = l16 >> 2, pp = lane & 3;
    const int ra_ = 8 * g + q, rb_ = ra_ + 4;
    const int sa = (ra_ >> 1) & 7, sb = (rb_ >> 1) & 7;
    LAS char* pa = lds + LA_XS + (w >> 1) * 2048;
    const int ua = 4 * (w & 1) + pp;
    f32x4 kv0 = {0, 0, 0, 0}, kv1 = {0, 0, 0, 0};
#pragma unroll
    for (int kt = 0; kt < 4; ++kt) {
      const u32x4 A = cat(tr_read(pa + kt * 4096 + ra_ * 64 + 8 * (ua ^ sa)), tr_read(pa + kt * 4096 + rb_ * 64 + 8 * (ua ^ sb)));
      LAS char* vb = lds + LA_V + kt * 2048;
      const u32x4 B0 = cat(tr_read(vb + ra_ * 64 + 8 * (pp ^ sa)), tr_read(vb + rb_ * 64 + 8 * (pp ^ sb)));
      const u32x4 B1 = cat(tr_read(vb + ra_ * 64 + 8 * ((4 + pp) ^ sa)), tr_read(vb + rb_ * 64 + 8 * ((4 + pp) ^ sb)));
      kv0 = mfma16(A, B0, kv0);
      kv1 = mfma16(A, B1, kv1);
    }
    const u32 base = WS_G1 + (u32)((h * 16 + rr) * 8 + 2 * w) * 1024 + lane * 16;
    const u32 t8 = tag & 0xffu;
    st16_sc1(rsws, base, (u32x4){(f2u(kv0[0]) & ~0xffu) | t8, (f2u(kv0[1]) & ~0xffu) | t8, (f2u(kv0[2]) & ~0xffu) | t8, (f2u(kv0[3]) & ~0xffu) | t8});
    st16_sc1(rsws, base + 1024, (u32x4){(f2u(kv1[0]) & ~0xffu) | t8, (f2u(kv1[1]) & ~0xffu) | t8, (f2u(kv1[2]) & ~0xffu) | t8, (f2u(kv1[3]) & ~0xffu) | t8});
  }
  TS(6);
  u32x4 pv[2];
  const u32 g1v = (u32)(lane >> 5) * 8192 + (u32)((lane >> 4) & 1) * 512 + (u32)l16 * 16;
  const u32 g1s = WS_G1 + (u32)(h * 16 + 4 * w) * 8192 + (u32)(rr >> 1) * 1024 + (u32)(rr & 1) * 256;
#pragma unroll
  for (int s = 0; s < 2; ++s) ald16s(pv[s], rsws, g1v, g1s + s * 16384);
  waitvm<6>();
  __builtin_amdgcn_s_barrier();
  asm volatile("" ::: "memory");
  const bool doF = o < 28;
  const int frg = 4 * xcd + o / 7, fcg = o % 7;
  const int C0 = 64 * fcg;
  const rsrc_t rsO = mkrs(p.wo, doF ? D * D * 2 : 0);
  const u32 wovoff = (u32)(((lane >> 3) * D + C0) * 2 + 16 * (((((lane & 7) >> 1) ^ ((lane >> 4) & 3)) << 1) | (lane & 1)));
  f32x16 aq = z16;
#pragma unroll
  for (int ks = 0; ks < 28; ++ks) {
    const u32x4 wq = cat(tr_read(wb + LA_WQ + ks * 1024), tr_read(wb + LA_WQ + ks * 1024 + 256));
    aq = mfma32(wq, xf[ks], aq);
    if ((ks & 3) == 3) { const int blk = 28 + w + 4 * (ks >> 2); dma16n(ldsb + LA_WO + blk * 1024, wovoff, rsO, blk * 8 * D * 2); }
  }
  TSX(7);
  LAS char* red = lds + LA_XS + 4096;
  LAS char* kvi = lds + LA_XS + 8192;
  {
    asm volatile("s_waitcnt vmcnt(7)" : "+v"(pv[0]), "+v"(pv[1]) :: "memory");
    auto pv_ok = [&]() {
      u32 m = (pv[0][0] ^ tag) | (pv[0][1] ^ tag) | (pv[0][2] ^ tag) | (pv[0][3] ^ tag);
      m |= (pv[1][0] ^ tag) | (pv[1][1] ^ tag) | (pv[1][2] ^ tag) | (pv[1][3] ^ tag);
      return __all((m & 0xffu) == 0); };
    if (!pv_ok()) {
      u32 spins = 0;
#pragma unroll 1
      do {
        if (spin_fail(spins, rsws)) break;
#pragma unroll
        for (int s = 0; s < 2; ++s) pv[s] = ld16_sc1(rsws, g1s + s * 16384 + g1v);
      } while (!pv_ok());
    }
    f32x4 a;
#pragma unroll
    for (int r = 0; r < 4; ++r) { const u32 x0 = pv[0][r], x1 = pv[1][r]; a[r] = u2f(x0) + u2f(x1); }
    *(LAS f32x4*)(red + w * 1024 + lane * 16) = a;
    bar();
    if (w == 0) {
      f32x4 t = (a + *(LAS f32x4*)(red + 1024 + lane * 16)) + (*(LAS f32x4*)(red + 2048 + lane * 16) + *(LAS f32x4*)(red + 3072 + lane * 16));
      float tt[4];
#pragma unroll
      for (int r = 0; r < 4; ++r) { float x, y; const float tr = t[r]; swap32(tr, x, y); tt[r] = x + y; }
      const u32 d0 = sane(pk_bf16(tt[0], tt[1])), d1 = sane(pk_bf16(tt[2], tt[3]));
      float a0, b0, a1, b1;
      swap16(u2f(d0), a0, b0); swap16(u2f(d1), a1, b1);
      st16_sc1(rsws, g2v, (u32x4){f2u(a0), f2u(a1), f2u(b0), f2u(b1)});
    }
    if (tid == 64) st4_sc1(rsws, WS_SEEN + 1024 + (u32)(rr * 16 + h) * 4, tag);
  }
  TSX(8);
  const u32 goff = WS_G2 + (u32)h * 4096 + (u32)w * 1024 + (u32)lane * 16;
  u32x4 kt;
  ald16s(kt, rsws, (u32)lane * 16, WS_G2 + (u32)h * 4096 + (u32)w * 1024);
  const u32 floff = WS_SEEN + (u32)(lane < 16 ? 256 + (frg >> 1) * 16 + lane : C0 / 2 + (lane & 31)) * 4;
  u32 flg = tag;
  u32x4 fp[2], fn[2];
  {
    u32x4 q0 = pack8(aq, 0), q1 = pack8(aq, 1);
    if (hi) { q1[2] = 0; q1[3] = 0; }
    f32x16 pq = mfma32(wf[0], q0, z16);
    pq = mfma32(wf[1], q1, pq);
    softmax32(pq, hi, fp, fn);
  }
  tie(fp[0]); tie(fp[1]); tie(fn[0]); tie(fn[1]);
#pragma unroll
  for (int i = 0; i < 7; ++i) { const int blk = w + 4 * i; dma16(ldsb + LA_WO + blk * 1024, wovoff, rsO, blk * 8 * D * 2); }
  TSX(9);
  {
    waitv<7>(kt);
    if (__any(max4(kt) == SENT)) {
      u32 spins = 0;
#pragma unroll 1
      do {
        if (spin_fail(spins, rsws)) break;
        kt = ld16_sc1(rsws, goff);
      } while (__any(max4(kt) == SENT));
    }
    *(LAS u32x4*)(kvi + w * 1024 + lane * 16) = kt;
    if (doF) flg = ld4_sc1(rsws, floff);
    bar();
    TSX(10);
    u32x4 kvf[4];
#pragma unroll
    for (int f = 0; f < 4; ++f) kvf[f] = *(LAS u32x4*)(kvi + f * 1024 + lane * 16);
    f32x16 n = mfma32(kvf[0], fp[0], z16);
    f32x16 n2 = mfma32(kvf[2], fn[0], z16);
    n = mfma32(kvf[1], fp[1], n);
    n2 = mfma32(kvf[3], fn[1], n2);
    n += n2;
    float a, b; swap32(n[12], a, b);
    const float id = rcpf_(rbf(rbf(b) + 1e-6f));
    u32x4 of[2];
#pragma unroll
    for (int s = 0; s < 2; ++s)
#pragma unroll
      for (int k = 0; k < 4; ++k) of[s][k] = sane(pk_bf16(rbf(n[8 * s + 2 * k]) * id, rbf(n[8 * s + 2 * k + 1]) * id));
    if (hi) { of[1][2] = 0; of[1][3] = 0; }
    st16_sc1(rsws, ob, of[0]); st16_sc1(rsws, ob + 1024, of[1]);
  }
  TSX(11);
  if (doF) {
    if (!__all(flg == tag)) {
      u32 spins = 0;
#pragma unroll 1
      do {
        if (spin_fail(spins, rsws)) break;
        flg = ld4_sc1(rsws, floff);
      } while (!__all(flg == tag));
    }
    const int R0 = frg * 64 + 16 * w;
    const int rt16 = R0 >> 4;
    const int l = R0 + l16;
    constexpr int ORD[16] = {0, 1, 3, 5, 7, 8, 10, 12, 14, 15, 2, 4, 6, 9, 11, 13};
    u32x4 of[16];
    const u32 ovoff = (u32)(((rt16 >> 1) * 16 * 2 + (g >> 1)) * 64 + l16 + 16 * (rt16 & 1) + 32 * (g & 1)) * 16;
    u32x2 gv;
    u32 bv[8];
    const u32 bvoff = WS_YB + (u32)(C0 / 2 + 2 * g) * YBS + (u32)l * 4;
#pragma unroll
    for (int k = 0; k < 16; ++k) ald16s(of[k], rsws, ovoff, WS_OF + ORD[k] * 2048);
    ald8s(gv, rsws, (u32)l * 8, WS_G);
#pragma unroll
    for (int i = 0; i < 8; ++i) asm volatile("buffer_load_dword %0, %1, %2, %3 offen sc1" : "=v"(bv[i]) : "v"(bvoff), "s"(rsws), "s"((u32)((i >> 1) * 8 + (i & 1)) * YBS) : "memory");
    bar();
    f32x4 ac[4][4];
    const int q = l16 >> 2, pp = lane & 3;
    LAS char* wob[4];
#pragma unroll
    for (int ct = 0; ct < 4; ++ct) {
      const int row0 = 16 * (g >> 1) + 4 * (g & 1) + q;
      wob[ct] = lds + LA_WO + row0 * 128 + ((ct ^ ((row0 >> 1) & 3)) << 5) + 8 * pp;
      asm volatile("" : "+v"(wob[ct]));
    }
    auto wfrag = [&](int hd, int ct) { LAS char* wp = wob[ct ^ (2 * (hd & 1))] + 3584 * hd; return cat(tr_read(wp), tr_read(wp + 1024)); };
#define FGROUP(gr) do { \
      _Pragma("unroll") for (int ct = 0; ct < 4; ++ct) ac[gr][ct] = (f32x4){0, 0, 0, 0}; \
      _Pragma("unroll") for (int k = 4 * (gr); k < 4 * (gr) + 4; ++k) { \
        u32x4 wf4[4]; \
        _Pragma("unroll") for (int ct = 0; ct < 4; ++ct) wf4[ct] = wfrag(ORD[k], ct); \
        _Pragma("unroll") for (int ct = 0; ct < 4; ++ct) ac[gr][ct] = mfma16(wf4[ct], of[k], ac[gr][ct]); \
      } \
      __builtin_amdgcn_sched_group_barrier(0x100, 16, 0); __builtin_amdgcn_sched_group_barrier(0x8, 4, 0); \
      __builtin_amdgcn_sched_group_barrier(0x100, 8, 0); __builtin_amdgcn_sched_group_barrier(0x8, 4, 0); \
      __builtin_amdgcn_sched_group_barrier(0x100, 8, 0); __builtin_amdgcn_sched_group_barrier(0x8, 8, 0); \
    } while (0)
#define RELOAD(k) asm volatile("buffer_load_dwordx4 %0, %1, %2, %3 offen sc1" : "+v"(of[k]) : "v"(ovoff), "s"(rsws), "s"(WS_OF + ORD[k] * 2048) : "memory")
    u32 bad = 0, gdone = 0;
#define FPASS1(gr) do { \
      waitv4<12 - 4 * (gr) + 9>(of[4 * (gr)], of[4 * (gr) + 1], of[4 * (gr) + 2], of[4 * (gr) + 3]); \
      _Pragma("unroll") for (int k = 4 * (gr); k < 4 * (gr) + 4; ++k) if (__any(max4(of[k]) == SENT)) { bad |= 1u << k; RELOAD(k); } \
      if ((bad & (0xfu << (4 * (gr)))) == 0) { FGROUP(gr); gdone |= 1u << (gr); } \
    } while (0)
    FPASS1(0); FPASS1(1); FPASS1(2); FPASS1(3);
    bad |= 0x10000u;
    {
      u32 spins = 0;
#pragma unroll 1
      for (;;) {
        asm volatile("s_waitcnt vmcnt(0)" : "+v"(of[0]), "+v"(of[1]), "+v"(of[2]), "+v"(of[3]), "+v"(of[4]), "+v"(of[5]), "+v"(of[6]), "+v"(of[7]),
                     "+v"(of[8]), "+v"(of[9]), "+v"(of[10]), "+v"(of[11]), "+v"(of[12]), "+v"(of[13]), "+v"(of[14]), "+v"(of[15]),
                     "+v"(gv), "+v"(bv[0]), "+v"(bv[1]), "+v"(bv[2]), "+v"(bv[3]), "+v"(bv[4]), "+v"(bv[5]), "+v"(bv[6]), "+v"(bv[7]) :: "memory");
        u32 nb = 0;
#pragma unroll
        for (int k = 0; k < 16; ++k) if ((bad & (1u << k)) && __any(max4(of[k]) == SENT)) { nb |= 1u << k; RELOAD(k); }
        if (bad & 0x10000u) {
          const u32 m = max(max(max(bv[0], bv[1]), max(bv[2], bv[3])), max(max(bv[4], bv[5]), max(bv[6], bv[7])));
          if (__any((gv[1] != tag) | (m == SENT))) {
            nb |= 0x10000u;
            asm volatile("buffer_load_dwordx2 %0, %1, %2, %3 offen sc1" : "+v"(gv) : "v"((u32)l * 8), "s"(rsws), "s"(WS_G) : "memory");
#pragma unroll
            for (int i = 0; i < 8; ++i) asm volatile("buffer_load_dword %0, %1, %2, %3 offen sc1" : "+v"(bv[i]) : "v"(bvoff), "s"(rsws), "s"((u32)((i >> 1) * 8 + (i & 1)) * YBS) : "memory");
          }
        }
        bad = nb;
        if (!(gdone & 1u) && (bad & 0x000fu) == 0) { FGROUP(0); gdone |= 1u; }
        if (!(gdone & 2u) && (bad & 0x00f0u) == 0) { FGROUP(1); gdone |= 2u; }
        if (!(gdone & 4u) && (bad & 0x0f00u) == 0) { FGROUP(2); gdone |= 4u; }
        if (!(gdone & 8u) && (bad & 0xf000u) == 0) { FGROUP(3); gdone |= 8u; }
        if (bad == 0) break;
        if (spin_fail(spins, rsws)) { asm volatile("s_waitcnt vmcnt(0)" ::: "memory"); break; }
      }
    }
    TS(12);
    f32x4 acc[4];
#pragma unroll
    for (int ct = 0; ct < 4; ++ct) acc[ct] = (ac[0][ct] + ac[1][ct]) + (ac[2][ct] + ac[3][ct]);
    TS(13);
    const u32 gu = gv[0];
    const float gg = u2f(gu);
    const float g1 = rbf(1.f - gg);
#pragma unroll
    for (int ct = 0; ct < 4; ++ct) {
      const u32 b01 = bv[2 * ct], b23 = bv[2 * ct + 1];
      const float bs[4] = {bf2f(b01), bfhi(b01), bf2f(b23), bfhi(b23)};
      float ov[4];
#pragma unroll
      for (int r = 0; r < 4; ++r) ov[r] = rbf(gg * rbf(acc[ct][r])) + rbf(g1 * bs[r]);
      *(u32x2*)(p.out + (size_t)l * D + C0 + 16 * ct + 4 * g) = (u32x2){pk_bf16(ov[0], ov[1]), pk_bf16(ov[2], ov[3])};
    }
  }
  TS(14);
  TS_OUT();
}

constexpr int CPS = 4160;
constexpr int CPCH = 4 * CPS;
constexpr int LB_CP = 0;
constexpr int LB_U = 2 * CPCH;
constexpr int LB_KC = LB_U + 2 * 4608;
constexpr int LB_MISC = LB_KC + 2 * 1536;
constexpr int LB_EX = LB_MISC + 256;
constexpr int LB_STG = 0;
constexpr int LB_TT = LB_EX + 8192;
constexpr int LB_KT = LB_EX;
static_assert(LB_EX + 24576 <= LDS_BYTES, "lds");
static_assert(LB_EX % 16 == 0, "lds");
struct cpx2 { f32x2 re, im; };
DI cpx2 cmul(const cpx2& a, const cpx2& b) { cpx2 r; r.re = a.re * b.re - a.im * b.im; r.im = a.re * b.im + a.im * b.re; return r; }
DI cpx2 csqr(const cpx2& a) { cpx2 r; r.re = a.re * a.re - a.im * a.im; r.im = (a.re + a.re) * a.im; return r; }
DI cpx2 cexp_m(f32x2 al, f32x2 th, float m) {
  cpx2 r;
#pragma unroll
  for (int i = 0; i < 2; ++i) {
    const float mag = exp2f_(al[i] * m);
    const float ph = __builtin_amdgcn_fractf(th[i] * m);
    r.re[i] = mag * __builtin_amdgcn_cosf(ph); r.im[i] = mag * __builtin_amdgcn_sinf(ph);
  }
  return r;
}
DI void put_row(LAS char* stg, int i, const cpx2& v) { *(LAS u32x2*)(stg + i * 544) = (u32x2){pk_f16(v.re[0], v.im[0]), pk_f16(v.re[1], v.im[1])}; }
DI float pow2_scale(float mx, int target) {
  const int e = (int)((__builtin_bit_cast(u32, mx) >> 23) & 0xff);
  int se = 254 + target - e;
  se = se < 1 ? 1 : (se > 254 ? 254 : se);
  return __builtin_bit_cast(float, (u32)se << 23);
}
DI u32 kpos(int l) {
  const u32 r = (pk_bf16((float)l, 0.f) & 0xffffu) - 17024u;
  const u32 v = l < 128 ? (u32)l : r;
  return (l < 0 ? 641u : v) * 2u;
}

DI void roleB(const Params& p, int xcd, int o, LAS char* lds, u32 ev, rsrc_t rsws) {
  TS_DECL;
  TS(0);
  const int tid = threadIdx.x, lane = tid & 63, w = __builtin_amdgcn_readfirstlane(tid >> 6);
  const int l16 = lane & 15, g = lane >> 4;
  const int pair = xcd * 28 + o;
  const int d0 = 2 * pair;
  const int ch = w >> 1, half = w & 1, d = d0 + ch;
  LAS float* misc = (LAS float*)(lds + LB_MISC);
  SETPRIO(3);
  const int pi = (d * NS + half * 128 + 2 * lane) >> 1;
  const u32 are = ((const u32*)p.Are)[pi], aim = ((const u32*)p.Aim)[pi], bre = ((const u32*)p.Bre)[pi], bim = ((const u32*)p.Bim)[pi], cre = ((const u32*)p.Cre)[pi], cim = ((const u32*)p.Cim)[pi];
  const float ldt = bf2f(p.logdt[d]);
  const u32 dsk2 = ((const u32*)p.dskip)[pair];
  const int tt_ = (xcd & 1) * 28 + o;
  const int trt = 8 * (xcd >> 1) + tt_ / 7, tcg = tt_ % 7;
  u32x4 tx[2];
  {
    const rsrc_t rsX = mkrs(p.x, S * D * 2);
#pragma unroll
    for (int i = 0; i < 2; ++i) ald16(tx[i], rsX, (u32)((tid >> 3) * D * 2 + 16 * (tid & 7)), (u32)((64 * trt + 32 * i) * D * 2 + 128 * tcg));
  }
#define YRST_POS 3
#define T_POS 0
  const u32 yb0 = WS_YB + (u32)pair * YBS + (u32)tid * 16;
  auto yreset = [&]() { st16_sc1(rsws, yb0, (u32x4){SENT, SENT, SENT, SENT}); st16_sc1(rsws, yb0 + 4096, (u32x4){SENT, SENT, SENT, SENT}); asm volatile("" ::: "memory"); };
  if (YRST_POS == 0) yreset();
  u32 ko[11];
#pragma unroll
  for (int e = 0; e < 11; ++e) ko[e] = LB_KC + kpos(2047 - 8 * tid - e);
  u32 tag;
  auto tphase = [&]() {
    asm volatile("s_waitcnt vmcnt(%2)" : "+v"(tx[0]), "+v"(tx[1]) : "n"(YRST_POS == 0 ? 2 : 0) : "memory");
    LAS char* tt = lds + LB_TT;
#pragma unroll
    for (int i = 0; i < 2; ++i) { const int row = 32 * i + (tid >> 3); *(LAS u32x4*)(tt + row * 128 + (((tid & 7) ^ (row & 7)) << 4)) = tx[i]; }
    u32 e2 = ev;
    asm volatile("" : "+v"(e2));
    tag = (u32)__builtin_amdgcn_readfirstlane(e2) + 1u;
    bar();
    const int q = l16 >> 2, pp = lane & 3;
#pragma unroll
    for (int i = 0; i < 2; ++i) {
      const int lc = g + 4 * i;
      const int ra = 8 * lc + q, rb = ra + 4;
      const int chunk = 2 * w + (pp >> 1), sub = (pp & 1) * 8;
      const u32x2 lo = tr_read(tt + ra * 128 + ((chunk ^ (ra & 7)) << 4) + sub);
      const u32x2 hi = tr_read(tt + rb * 128 + ((chunk ^ (rb & 7)) << 4) + sub);
      const u32 a0 = WS_XT + (u32)trt * XTS + (u32)(64 * tcg + 16 * w + l16) * 256 + (u32)(8 * i + g) * 16;
      st16_sc1(rsws, a0, (u32x4){lo.x, tag, lo.y, tag});
      st16_sc1(rsws, a0 + 64, (u32x4){hi.x, tag, hi.y, tag});
    }
    asm volatile("" ::: "memory");
  };
  if (T_POS == 0) tphase();
  if (YRST_POS == 1) yreset();
  SETPRIO(0);
  TS(1);
  const float dt = rbf(__expf(ldt));
  const f32x2 al = (f32x2){bf2f(are), bfhi(are)} * (dt * LOG2E);
  const f32x2 th = (f32x2){bf2f(aim), bfhi(aim)} * dt * INV2PI;
  const f32x2 br = (f32x2){bf2f(bre), bfhi(bre)} * dt, bi = (f32x2){bf2f(bim), bfhi(bim)} * dt;
  const f32x2 cr = {bf2f(cre), bfhi(cre)}, ci = {bf2f(cim), bfhi(cim)};
  cpx2 W; W.re = cr * br - ci * bi; W.im = cr * bi + ci * br;
  const float wm0 = __builtin_amdgcn_sqrtf(W.re[0] * W.re[0] + W.im[0] * W.im[0]) * exp2f_(fmaxf(0.f, al[0] * 2048.f));
  const float wm1 = __builtin_amdgcn_sqrtf(W.re[1] * W.re[1] + W.im[1] * W.im[1]) * exp2f_(fmaxf(0.f, al[1] * 2048.f));
  const float wmax = wave_max(fmaxf(wm0, wm1));
  const float SR = pow2_scale(wmax, 14);
  W.re *= SR; W.im *= SR;
  LAS char* stgr = lds + LB_STG + w * 8704;
  LAS char* stg = stgr + lane * 8;
  LAS char* frd = stgr + l16 * 544 + 16 * g;
  {
    const cpx2 Z8 = cexp_m(al, -th, 8.f);
    const cpx2 Z16 = csqr(Z8), Z32 = csqr(Z16), Z64 = csqr(Z32);
    cpx2 C[8];
    C[0].re = (f32x2){1.f, 1.f}; C[0].im = (f32x2){0.f, 0.f};
    C[1] = Z8; C[2] = Z16; C[3] = cmul(Z8, Z16); C[4] = Z32; C[5] = cmul(Z32, Z8); C[6] = cmul(Z32, Z16); C[7] = cmul(Z32, C[3]);
#pragma unroll
    for (int j = 0; j < 8; ++j) put_row(stg, j, C[j]);
    put_row(stg, 8, Z64);
#pragma unroll
    for (int j = 1; j < 8; ++j) put_row(stg, 8 + j, cmul(Z64, C[j]));
  }
  u32x4 Bf[8], Af[8];
#pragma unroll
  for (int ks = 0; ks < 8; ++ks) Bf[ks] = *(LAS u32x4*)(frd + 64 * ks);
  __builtin_amdgcn_sched_barrier(0);
  TS(2);
  if (T_POS == 1) tphase();
  if (YRST_POS == 2) yreset();
  const cpx2 Z1 = cexp_m(al, th, 1.f);
  const cpx2 Z2 = csqr(Z1), Z4 = csqr(Z2);
  cpx2 Sm[8];
  Sm[1] = Z1; Sm[2] = Z2; Sm[3] = cmul(Z1, Z2); Sm[4] = Z4; Sm[5] = cmul(Z4, Z1); Sm[6] = cmul(Z4, Z2); Sm[7] = cmul(Z4, Sm[3]);
  const cpx2 Z128 = cexp_m(al, th, 128.f);
  const cpx2 Z256 = csqr(Z128), Z512 = csqr(Z256);
  f32x4 kacc[3];
  auto kmma = [&]() {
    f32x4 a0 = {0, 0, 0, 0}, a1 = {0, 0, 0, 0};
#pragma unroll
    for (int ks = 0; ks < 8; ks += 2) { a0 = mfma_h(Af[ks], Bf[ks], a0); a1 = mfma_h(Af[ks + 1], Bf[ks + 1], a1); }
    return a0 + a1;
  };
  auto rdA = [&]() {
#pragma unroll
    for (int ks = 0; ks < 8; ++ks) Af[ks] = *(LAS u32x4*)(frd + 64 * ks);
    __builtin_amdgcn_sched_barrier(0);
  };
  {
    const cpx2 B1 = cmul(W, Z128);
    put_row(stg, 0, W);
#pragma unroll
    for (int i = 1; i < 8; ++i) put_row(stg, i, cmul(W, Sm[i]));
    put_row(stg, 8, B1);
#pragma unroll
    for (int i = 1; i < 8; ++i) put_row(stg, 8 + i, cmul(B1, Sm[i]));
  }
  rdA();
  const cpx2 Z384 = cmul(Z256, Z128);
  {
    const cpx2 B2 = cmul(W, Z256), B3 = cmul(W, Z384), B4 = cmul(W, Z512);
    const cpx2 B5 = cmul(B4, Z128), B6 = cmul(B4, Z256), B7 = cmul(B4, Z384);
    put_row(stg, 0, B2); put_row(stg, 1, cmul(B2, Sm[2])); put_row(stg, 2, cmul(B2, Sm[4])); put_row(stg, 3, cmul(B2, Sm[6]));
    put_row(stg, 4, B3); put_row(stg, 5, cmul(B3, Sm[2])); put_row(stg, 6, cmul(B3, Sm[4])); put_row(stg, 7, cmul(B3, Sm[6]));
    put_row(stg, 8, B4); put_row(stg, 9, cmul(B4, Sm[4])); put_row(stg, 10, B5); put_row(stg, 11, cmul(B5, Sm[4]));
    put_row(stg, 12, B6); put_row(stg, 13, cmul(B6, Sm[4])); put_row(stg, 14, B7); put_row(stg, 15, cmul(B7, Sm[4]));
    kacc[0] = kmma();
#pragma unroll
    for (int i = 0; i < 8; ++i) { __builtin_amdgcn_sched_group_barrier(0x8, 1, 0); __builtin_amdgcn_sched_group_barrier(0x202, 14, 0); }
  }
  rdA();
  TS(3);
  if (T_POS == 2) tphase();
  if (YRST_POS == 3) yreset();
  TS(4);
  {
    const cpx2 Z1024 = cexp_m(al, th, 1024.f);
    const cpx2 Z2048 = csqr(Z1024);
    const cpx2 B8 = cmul(W, Z1024);
    const cpx2 Z640 = cmul(Z512, Z128), Z768 = cmul(Z512, Z256), Z896 = cmul(Z512, Z384);
    put_row(stg, 0, B8); put_row(stg, 1, cmul(B8, Z128)); put_row(stg, 2, cmul(B8, Z256)); put_row(stg, 3, cmul(B8, Z384));
    put_row(stg, 4, cmul(B8, Z512)); put_row(stg, 5, cmul(B8, Z640)); put_row(stg, 6, cmul(B8, Z768)); put_row(stg, 7, cmul(B8, Z896));
    put_row(stg, 8, cmul(W, Z2048));
    kacc[1] = kmma();
#pragma unroll
    for (int i = 0; i < 8; ++i) { __builtin_amdgcn_sched_group_barrier(0x8, 1, 0); __builtin_amdgcn_sched_group_barrier(0x202, 12, 0); }
  }
  rdA();
  kacc[2] = kmma();
  u32x4 uv[4];
  const u32 uoff = WS_XT + (u32)(16 * half) * XTS + (u32)d * 256;
  const u32 uvo = (u32)(lane >> 4) * XTS + (u32)l16 * 16;
  {
    const float isr = rcpf_(SR);
    LAS char* kx = lds + LB_KT + w * 2048 + lane * 16;
    if (half == 0) { *(LAS f32x4*)(kx) = kacc[1] * isr; }
    else { *(LAS f32x4*)(kx) = kacc[0] * isr; *(LAS f32x4*)(kx + 1024) = kacc[2] * isr; }
    TS(5);
#pragma unroll
    for (int i = 0; i < 4; ++i) ald16s(uv[i], rsws, uvo, uoff + i * 4 * XTS);
    waitvm<4>();
    bar();
  }
  if (tid == 0) st4_sc1(rsws, WS_SEEN + (u32)pair * 4, tag);
  {
    const float isr = rcpf_(SR);
    LAS char* kx = lds + LB_KT + (w ^ 1) * 2048 + lane * 16;
    LAS char* kc = lds + LB_KC + ch * 1536;
    if (half == 0) {
      const f32x4 t0 = kacc[0] * isr + *(LAS f32x4*)(kx);
      const f32x4 t2 = kacc[2] * isr + *(LAS f32x4*)(kx + 1024);
      *(LAS u32x2*)(kc + (4 * (g & 1) + 8 * l16 + 128 * (g >> 1)) * 2) = (u32x2){pk_bf16(t0[0], t0[1]), pk_bf16(t0[2], t0[3])};
      const u32 h01 = pk_bf16(t2[0], t2[1]), h23 = pk_bf16(t2[2], t2[3]);
      if (g < 2) {
        LAS char* q2 = kc + (512 + l16 + 64 * g) * 2;
        *(LAS u16*)(q2) = (u16)h01; *(LAS u16*)(q2 + 32) = (u16)(h01 >> 16); *(LAS u16*)(q2 + 64) = (u16)h23; *(LAS u16*)(q2 + 96) = (u16)(h23 >> 16);
      } else if (lane == 32) { *(LAS u32*)(kc + 640 * 2) = h01 & 0xffffu; }
    } else {
      const f32x4 t1 = kacc[1] * isr + *(LAS f32x4*)(kx);
      const u32 h01 = pk_bf16(t1[0], t1[1]), h23 = pk_bf16(t1[2], t1[3]);
      if (g < 2) *(LAS u32x2*)(kc + (256 + 4 * l16 + 64 * g) * 2) = (u32x2){h01, h23};
      else { LAS char* q1 = kc + (384 + 2 * l16 + 64 * (g - 2)) * 2; *(LAS u32*)(q1) = h01; *(LAS u32*)(q1 + 64) = h23; }
    }
  }
  bar();
  TS(6);
  {
    u32 dw[2][6];
#pragma unroll
    for (int c = 0; c < 2; ++c) {
#pragma unroll
      for (int k = 0; k < 5; ++k) {
        u16x2 v;
        v.x = *(LAS u16*)(lds + c * 1536 + ko[2 * k]);
        v.y = *(LAS u16*)(lds + c * 1536 + ko[2 * k + 1]);
        dw[c][k] = __builtin_bit_cast(u32, v);
      }
      dw[c][5] = *(LAS u16*)(lds + c * 1536 + ko[10]);
    }
#pragma unroll
    for (int c = 0; c < 2; ++c) {
      u32 od[5];
#pragma unroll
      for (int k = 0; k < 5; ++k) od[k] = __builtin_amdgcn_alignbit(dw[c][k + 1], dw[c][k], 16);
      LAS char* cp = lds + LB_CP + c * CPCH + tid * 16;
      *(LAS u32x4*)(cp) = (u32x4){dw[c][0], dw[c][1], dw[c][2], dw[c][3]};
      *(LAS u32x4*)(cp + CPS) = (u32x4){od[0], od[1], od[2], od[3]};
      *(LAS u32x4*)(cp + 2 * CPS) = (u32x4){dw[c][1], dw[c][2], dw[c][3], dw[c][4]};
      *(LAS u32x4*)(cp + 3 * CPS) = (u32x4){od[1], od[2], od[3], od[4]};
    }
  }
  if (tid < 32) *(LAS u32x4*)(lds + LB_CP + (tid >> 2) * CPS + 4096 + (tid & 3) * 16) = (u32x4){0, 0, 0, 0};
  TS(7);
  {
    waitv4<0>(uv[0], uv[1], uv[2], uv[3]);
    auto uv_ok = [&]() { bool ok = true;
#pragma unroll
      for (int i = 0; i < 4; ++i) ok &= (uv[i][1] == tag) & (uv[i][3] == tag);
      return __all(ok); };
    if (!uv_ok()) {
      u32 spins = 0;
#pragma unroll 1
      do {
        if (spin_fail(spins, rsws)) break;
#pragma unroll
        for (int i = 0; i < 4; ++i) uv[i] = ld16_sc1(rsws, uoff + i * 4 * XTS + uvo);
      } while (!uv_ok());
    }
    LAS char* ub = lds + LB_U + ch * 4608;
    const int sg = lane & 15;
    const int pi4 = 8 * (sg >> 3) + 2 * (sg & 3) + ((sg >> 2) & 1);
    LAS char* uw = ub + 512 + half * 2048 + (64 * (lane >> 4) + 4 * pi4) * 2;
#pragma unroll
    for (int i = 0; i < 4; ++i) { const u32 a = uv[i][0], b = uv[i][2]; *(LAS u32x2*)(uw + i * 512) = (u32x2){a, b}; }
    if (half == 0 && lane < 32) *(LAS u32x4*)(ub + lane * 16) = (u32x4){0, 0, 0, 0};
  }
  bar();
  TS(8);
  f32x4 acc[8];
#pragma unroll
  for (int i = 0; i < 8; ++i) acc[i] = (f32x4){0, 0, 0, 0};
  {
    const int cs = (3 - l16) & 3;
    LAS char* abase = lds + LB_CP + ch * CPCH + cs * CPS + (2031 - l16 + 8 * g - cs) * 2 - 512 * 7 - 64 * (4 * half + 3);
    LAS char* bbase = lds + LB_U + ch * 4608 + (256 + 16 * (l16 - 1) + 8 * g) * 2 - 64 * (4 * half + 3);
    LAS char* abase2 = abase + 8;
    asm volatile("" : "+v"(abase2));
    u32x4 A[2][8], B[2][8];
    auto ldfr = [&](int it, int buf) {
#pragma unroll
      for (int s = 0; s < 8; ++s) {
        const int off = 512 * (7 - s) + 64 * (3 - it);
        A[buf][s] = cat(*(LAS u32x2*)(abase + off), *(LAS u32x2*)(abase2 + off));
        B[buf][s] = *(LAS u32x4*)(bbase + 512 * s + 64 * (3 - it));
      }
    };
    ldfr(0, 0);
    __builtin_amdgcn_sched_barrier(0);
#pragma unroll
    for (int it = 0; it < 4; ++it) {
      if (it < 3) ldfr(it + 1, (it + 1) & 1);
#pragma unroll
      for (int s = 0; s < 8; ++s)
#pragma unroll
        for (int cb = 7; cb >= s; --cb) acc[cb] = mfma16(A[it & 1][s], B[it & 1][cb - s], acc[cb]);
      if (it < 3) {
#pragma unroll
        for (int i = 0; i < 24; ++i) { __builtin_amdgcn_sched_group_barrier(0x8, 1, 0); __builtin_amdgcn_sched_group_barrier(0x100, 1, 0); }
      }
      __builtin_amdgcn_sched_barrier(0);
    }
  }
  TS(9);
  SETPRIO(3);
  {
    LAS char* ex = lds + LB_EX;
    f32x4 own0 = acc[0], own1 = acc[1];
#pragma unroll
    for (int pw = 0; pw < 4; ++pw) {
      if (pw != w) {
        LAS char* dst = ex + ((pw * 3 + (w > pw ? w - 1 : w)) * 2) * 1024 + lane * 16;
        *(LAS f32x4*)(dst) = acc[2 * pw]; *(LAS f32x4*)(dst + 1024) = acc[2 * pw + 1];
      } else { own0 = acc[2 * pw]; own1 = acc[2 * pw + 1]; }
    }
    const float sd0 = bf2f(dsk2), sd1 = bfhi(dsk2);
    float sk[2][2][4];
#pragma unroll
    for (int ci = 0; ci < 2; ++ci) {
      const int l0 = 256 * (2 * w + ci) + 16 * l16 + 4 * g;
      const u32x2 u0 = *(LAS u32x2*)(lds + LB_U + 512 + l0 * 2);
      const u32x2 u1 = *(LAS u32x2*)(lds + LB_U + 4608 + 512 + l0 * 2);
      const u32 a0 = u0[0], a1 = u0[1], b0 = u1[0], b1 = u1[1];
      sk[ci][0][0] = rbf(bf2f(a0) * sd0); sk[ci][0][1] = rbf(bfhi(a0) * sd0); sk[ci][0][2] = rbf(bf2f(a1) * sd0); sk[ci][0][3] = rbf(bfhi(a1) * sd0);
      sk[ci][1][0] = rbf(bf2f(b0) * sd1); sk[ci][1][1] = rbf(bfhi(b0) * sd1); sk[ci][1][2] = rbf(bf2f(b1) * sd1); sk[ci][1][3] = rbf(bfhi(b1) * sd1);
    }
    bar();
    f32x4 v0[4], v1[4];
#pragma unroll
    for (int sw = 0; sw < 4; ++sw) {
      if (sw != w) {
        LAS char* src = ex + ((w * 3 + (sw > w ? sw - 1 : sw)) * 2) * 1024 + lane * 16;
        v0[sw] = *(LAS f32x4*)(src); v1[sw] = *(LAS f32x4*)(src + 1024);
      } else { v0[sw] = own0; v1[sw] = own1; }
    }
#pragma unroll
    for (int ci = 0; ci < 2; ++ci) {
      const int cb = 2 * w + ci;
      const f32x4 y0 = ci ? v1[0] + v1[1] : v0[0] + v0[1];
      const f32x4 y1 = ci ? v1[2] + v1[3] : v0[2] + v0[3];
      u32 P[4];
#pragma unroll
      for (int r = 0; r < 4; ++r) P[r] = pk_bf16(rbf(y0[r]) + sk[ci][0][r], rbf(y1[r]) + sk[ci][1][r]);
      st16_sc1(rsws, WS_YB + (u32)pair * YBS + (u32)(256 * cb + 16 * l16 + 4 * g) * 4, (u32x4){sane(P[0]), sane(P[1]), sane(P[2]), sane(P[3])});
    }
  }
  TS(10);
  TS_OUT();
}

DI void roleG(const Params& p, int xcd, int o, u32 ev, rsrc_t rsws) {
  const int tid = threadIdx.x;
  const int gb = 8 * (xcd >> 1) + 4 * (xcd & 1) + (o - 28);
  const int row = gb * 64 + (tid >> 2), part = tid & 3;
  float s = 0.f;
#pragma unroll
  for (int i = 0; i < 14; ++i) {
    const u32x4 xv = *(const u32x4*)(p.x + (size_t)row * D + (i * 4 + part) * 8);
    const u32x4 gv = *(const u32x4*)(p.gatew + (i * 4 + part) * 8);
#pragma unroll
    for (int k = 0; k < 4; ++k) {
      const u32 r = pk_bf16(bf2f(xv[k]) * bf2f(gv[k]), bfhi(xv[k]) * bfhi(gv[k]));
      s += bf2f(r); s += bfhi(r);
    }
  }
  asm volatile("" : "+v"(ev));
  const u32 tag = (u32)__builtin_amdgcn_readfirstlane(ev) + 1u;
  if (tid == 0) st4_sc1(rsws, WS_SEEN + (u32)(224 + gb) * 4, tag);
  s += dpp<0xB1>(s); s += dpp<0x4E>(s);
  if (part == 0) { const float lg = rbf(s); st8_sc1(rsws, WS_G + (u32)row * 8, (u32x2){f2u(rbf(rcpf_(1.f + exp2f_(-lg * LOG2E)))), tag}); }
  if (gb == 0 && tid < 64) {
    u32 spins = 0;
#pragma unroll 1
    for (;;) {
      const u32x4 a = ld16_sc1(rsws, WS_SEEN + (u32)tid * 16), b = ld16_sc1(rsws, WS_SEEN + 1024 + (u32)tid * 16);
      const bool ok = (a[0] == tag) & (a[1] == tag) & (a[2] == tag) & (a[3] == tag) & (b[0] == tag) & (b[1] == tag) & (b[2] == tag) & (b[3] == tag);
      if (__all(ok) || spin_fail(spins, rsws)) break;
      __builtin_amdgcn_s_sleep(8);
    }
    if (tid == 0) st4_sc1(rsws, WS_EPOCH, tag);
  }
}

__global__ void __launch_bounds__(256, 2) k_main(Params p) {
  extern __shared__ __attribute__((aligned(16))) char smem[];
  LAS char* lds = (LAS char*)smem;
  const rsrc_t rsws = mkrs(p.ws, WS_USED);
  const int b = blockIdx.x, xcd = b & 7, j = b >> 3;
  const u32 ev = ld4_sc1(rsws, WS_EPOCH);
  asm volatile("" ::: "memory");
  const int o = j & 31;
  if (j < 32) { roleA(p, xcd, o, lds, ev, rsws); }
  else if (o < 28) { roleB(p, xcd, o, lds, ev, rsws); }
  else { roleG(p, xcd, o, ev, rsws); }
}

extern "C" void kernel_launch(void* const* d_in, const int* in_sizes, int n_in, void* d_out, int out_size, void* d_ws, size_t ws_size, hipStream_t stream) {
  Params p;
  p.x = (const u16*)d_in[0]; p.wqkv = (const u16*)d_in[1]; p.wphi = (const u16*)d_in[2]; p.wo = (const u16*)d_in[3];
  p.Are = (const u16*)d_in[5]; p.Aim = (const u16*)d_in[6]; p.Bre = (const u16*)d_in[7]; p.Bim = (const u16*)d_in[8];
  p.Cre = (const u16*)d_in[9]; p.Cim = (const u16*)d_in[10]; p.logdt = (const u16*)d_in[11]; p.dskip = (const u16*)d_in[12]; p.gatew = (const u16*)d_in[13];
  p.out = (u16*)d_out; p.ws = (char*)d_ws;
  (void)hipFuncSetAttribute((const void*)k_main, hipFuncAttributeMaxDynamicSharedMemorySize, LDS_BYTES);
  k_main<<<512, 256, LDS_BYTES, stream>>>(p);
}
```

```cpp
#include <hip/hip_runtime.h>
#include <stdint.h>

typedef unsigned short u16;
typedef unsigned int u32;
typedef unsigned long long u64;
typedef short s16x4 __attribute__((ext_vector_type(4)));
typedef _Float16 f16x8 __attribute__((ext_vector_type(8)));
typedef _Float16 f16x2 __attribute__((ext_vector_type(2)));
typedef _Float16 f16x4 __attribute__((ext_vector_type(4)));
typedef __bf16 bf16x8 __attribute__((ext_vector_type(8)));
typedef __bf16 bf16x2 __attribute__((ext_vector_type(2)));
typedef float f32x16 __attribute__((ext_vector_type(16)));
typedef float f32x4 __attribute__((ext_vector_type(4)));
typedef float f32x2 __attribute__((ext_vector_type(2)));
typedef u32 u32x4 __attribute__((ext_vector_type(4)));
typedef u32 u32x2 __attribute__((ext_vector_type(2)));
typedef u16 u16x2 __attribute__((ext_vector_type(2)));
#define SETPRIO(x) __builtin_amdgcn_s_setprio(x)
#define LAS __attribute__((address_space(3)))
#define DI __device__ __forceinline__
typedef __amdgpu_buffer_rsrc_t rsrc_t;

constexpr int S = 2048, D = 448, H = 16, DH = 28, NS = 256;
constexpr float LOG2E = 1.4426950408889634f;
constexpr float INV2PI = 0.15915494309189535f;

constexpr u32 WS_EPOCH = 0;
constexpr u32 WS_TMO   = 256;
constexpr u32 WS_SEEN  = 4096;
constexpr u32 WS_G     = 128u << 10;
constexpr u32 WS_G2    = 64u << 10;
constexpr u32 WS_G1    = 1u << 20;
constexpr u32 WS_OF    = 3u << 20;
constexpr u32 WS_XT    = 11u << 19;
constexpr u32 WS_YB    = 19u << 19;
constexpr u32 WS_USED  = 16u << 20;
#define OFPAD 0
#define OFHPAD 0
constexpr u32 OFH = 2048 + OFHPAD, OFS = 16 * OFH + OFPAD;
static_assert(WS_OF + 64 * OFS <= WS_XT, "the o region ends where XT begins");
constexpr u32 XTS = 449 * 256, YBS = 8192 + 128;
constexpr u32 SENT = 0xFFFFFFFFu;
constexpr u32 OOB = 0x40000000u;

struct Params {
  const u16 *x, *wqkv, *wphi, *wo, *Are, *Aim, *Bre, *Bim, *Cre, *Cim, *logdt, *dskip, *gatew;
  u16* out; char* ws;
};

DI u32 f2u(float v) { return __builtin_bit_cast(u32, v); }
DI float u2f(u32 v) { return __builtin_bit_cast(float, v); }
DI float bf2f(u32 v) { return __builtin_bit_cast(float, v << 16); }
DI float bfhi(u32 v) { return __builtin_bit_cast(float, v & 0xffff0000u); }
DI u32 pk_bf16(float a, float b) { f32x2 v = {a, b}; return __builtin_bit_cast(u32, __builtin_convertvector(v, bf16x2)); }
DI u32 pk_f16(float a, float b) { f32x2 v = {a, b}; return __builtin_bit_cast(u32, __builtin_convertvector(v, f16x2)); }
DI float rbf(float a) { return u2f(pk_bf16(0.f, a)); }
DI f32x4 mfma16(u32x4 a, u32x4 b, f32x4 c) { return __builtin_amdgcn_mfma_f32_16x16x32_bf16(__builtin_bit_cast(bf16x8, a), __builtin_bit_cast(bf16x8, b), c, 0, 0, 0); }
DI f32x16 mfma32(u32x4 a, u32x4 b, f32x16 c) { return __builtin_amdgcn_mfma_f32_32x32x16_bf16(__builtin_bit_cast(bf16x8, a), __builtin_bit_cast(bf16x8, b), c, 0, 0, 0); }
DI f32x4 mfma_h(u32x4 a, u32x4 b, f32x4 c) { return __builtin_amdgcn_mfma_f32_16x16x32_f16(__builtin_bit_cast(f16x8, a), __builtin_bit_cast(f16x8, b), c, 0, 0, 0); }
DI u32x2 tr_read(LAS char* p) { return __builtin_bit_cast(u32x2, __builtin_amdgcn_ds_read_tr16_b64_v4i16((LAS s16x4*)p)); }
DI u32x4 cat(u32x2 a, u32x2 b) { return (u32x4){a.x, a.y, b.x, b.y}; }
template <int CTRL> DI float dpp(float x) { return __builtin_bit_cast(float, __builtin_amdgcn_mov_dpp(__builtin_bit_cast(int, x), CTRL, 0xf, 0xf, true)); }
DI float row16_max(float x) { x = fmaxf(x, dpp<0xB1>(x)); x = fmaxf(x, dpp<0x4E>(x)); x = fmaxf(x, dpp<0x141>(x)); x = fmaxf(x, dpp<0x140>(x)); return x; }
DI float row16_sum(float x) { x += dpp<0xB1>(x); x += dpp<0x4E>(x); x += dpp<0x141>(x); x += dpp<0x140>(x); return x; }
DI void swap16(float x, float& a, float& b) { const int u = __builtin_bit_cast(int, x); auto r = __builtin_amdgcn_permlane16_swap(u, u, false, false); const int r0 = r[0], r1 = r[1]; a = __builtin_bit_cast(float, r0); b = __builtin_bit_cast(float, r1); }
DI void swap32(float x, float& a, float& b) { const int u = __builtin_bit_cast(int, x); auto r = __builtin_amdgcn_permlane32_swap(u, u, false, false); const int r0 = r[0], r1 = r[1]; a = __builtin_bit_cast(float, r0); b = __builtin_bit_cast(float, r1); }
DI float wave_max(float x) { float a, b; x = row16_max(x); swap16(x, a, b); x = fmaxf(a, b); swap32(x, a, b); return fmaxf(a, b); }
DI float wave_sum(float x) { float a, b; x = row16_sum(x); swap16(x, a, b); x = a + b; swap32(x, a, b); return a + b; }
DI float exp2f_(float x) { return __builtin_amdgcn_exp2f(x); }
DI float rcpf_(float x) { return __builtin_amdgcn_rcpf(x); }
DI rsrc_t mkrs(const void* p, u32 bytes) { return __builtin_amdgcn_make_buffer_rsrc((void*)p, 0, (int)bytes, 0x00020000); }
DI void dma16(u32 lds_addr, u32 voff, rsrc_t rs, u32 soff) {
  asm volatile("s_mov_b32 m0, %0\n\ts_nop 0\n\tbuffer_load_dwordx4 %1, %2, %3 offen lds" :: "s"(lds_addr), "v"(voff), "s"(rs), "s"(soff) : "memory");
}
DI void dma16n(u32 lds_addr, u32 voff, rsrc_t rs, u32 soff) {
  asm volatile("s_mov_b32 m0, %0\n\ts_nop 0\n\tbuffer_load_dwordx4 %1, %2, %3 offen lds" :: "s"(lds_addr), "v"(voff), "s"(rs), "s"(soff));
}
template <int N> DI void waitvm() { asm volatile("s_waitcnt vmcnt(%0)" :: "n"(N) : "memory"); }
DI void bar() { asm volatile("s_waitcnt lgkmcnt(0)" ::: "memory"); __builtin_amdgcn_s_barrier(); asm volatile("" ::: "memory"); }
constexpr int AUX_SC1 = 16;
DI u32x4 ld16_sc1(rsrc_t rs, u32 off) { return __builtin_amdgcn_raw_buffer_load_b128(rs, off, 0, AUX_SC1); }
DI u32x2 ld8_sc1(rsrc_t rs, u32 off) { return __builtin_amdgcn_raw_buffer_load_b64(rs, off, 0, AUX_SC1); }
DI u32 ld4_sc1(rsrc_t rs, u32 off) { return __builtin_amdgcn_raw_buffer_load_b32(rs, off, 0, AUX_SC1); }
DI void st16_sc1(rsrc_t rs, u32 off, u32x4 v) { __builtin_amdgcn_raw_buffer_store_b128(v, rs, off, 0, AUX_SC1); }
DI void st8_sc1(rsrc_t rs, u32 off, u32x2 v) { __builtin_amdgcn_raw_buffer_store_b64(v, rs, off, 0, AUX_SC1); }
DI void st4_sc1(rsrc_t rs, u32 off, u32 v) { __builtin_amdgcn_raw_buffer_store_b32(v, rs, off, 0, AUX_SC1); }
DI void ald16(u32x4& d, rsrc_t rs, u32 voff, u32 soff) { asm volatile("buffer_load_dwordx4 %0, %1, %2, %3 offen" : "=v"(d) : "v"(voff), "s"(rs), "s"(soff) : "memory"); }
DI void ald16s(u32x4& d, rsrc_t rs, u32 voff, u32 soff) { asm volatile("buffer_load_dwordx4 %0, %1, %2, %3 offen sc1" : "=v"(d) : "v"(voff), "s"(rs), "s"(soff) : "memory"); }
DI void ald8s(u32x2& d, rsrc_t rs, u32 voff, u32 soff) { asm volatile("buffer_load_dwordx2 %0, %1, %2, %3 offen sc1" : "=v"(d) : "v"(voff), "s"(rs), "s"(soff) : "memory"); }
template <int N> DI void waitv(u32x4& a) { asm volatile("s_waitcnt vmcnt(%1)" : "+v"(a) : "n"(N) : "memory"); }
template <int N> DI void waitv4(u32x4& a, u32x4& b, u32x4& c, u32x4& d) { asm volatile("s_waitcnt vmcnt(%4)" : "+v"(a), "+v"(b), "+v"(c), "+v"(d) : "n"(N) : "memory"); }
DI void tie(u32x4& a) { asm volatile("" : "+v"(a) :: "memory"); }
DI u32 max4(u32x4 v) { return max(max(v[0], v[1]), max(v[2], v[3])); }
DI u32 sane(u32 v) { return min(v, 0xFFFFFFFEu); }
constexpr u32 SPIN_CAP = 1u << 14;
DI bool spin_fail(u32& spins, rsrc_t rsws) {
  __builtin_amdgcn_s_sleep(2);
  asm volatile("" ::: "memory");
  if (++spins > SPIN_CAP) { st4_sc1(rsws, WS_TMO, 1u); return true; }
  return false;
}

#define FLD ald16s
constexpr int LDS_BYTES = 75776;
#define WOV 1
#define PVPOS 2
#define KSLEEP 0
#define CONVADJ 0
#define SMQ 1
#define KTPOS 0
#define FSLEEP 16
#define FSLEEPS FSLEEP
#define FSLEEP2 0
#define FSLEEP2S FSLEEP2
#define FCOMB 1
#define FPIN 1
#define FIL 1
#define APIN 1
#define WOL2 0
#define YPOS 1

constexpr int LA_WK = 0, LA_WV = 28672, LA_XS = 57344, LA_WP = 73728;
constexpr int LA_V = LA_WV, LA_WO = 0;
constexpr int wq_slot(int i) { return LA_WK + i * 4096; }
#define LOOKA 3
struct ASeq { int w[7]; };
#define ACC2 0
#define ACC2K 0
#define QLA 2
#define VNV 2
#define WQPOS 0
#define VMODE 0
#define VNV1 3
#define VLA 6
#define NWV4 4
#define NWV5 3
#define NWV3 0
constexpr int nwv(int c) { return c == 3 ? NWV3 : c == 4 ? NWV4 : c == 5 ? NWV5 : c == 6 ? 7 - NWV3 - NWV4 - NWV5 : 0; }
constexpr int nwv0(int c) { int n = 0; for (int i = 0; i < c; ++i) n += nwv(i); return n; }
constexpr ASeq make_aseq() {
  ASeq r{};
  int seq[128] = {}; int n = 0;
  for (int k = 0; k < LOOKA && k < 7; ++k) for (int e = 0; e < 5; ++e) seq[n++] = k;
  for (int c = 0; c < 7; ++c) {
    int last = -1;
    for (int i = 0; i < n; ++i) if (seq[i] == c) last = i;
    r.w[c] = n - 1 - last;
    if (c + LOOKA < 7) for (int e = 0; e < 5; ++e) seq[n++] = c + LOOKA;
    if (c == 0) { seq[n++] = -1; }
    for (int e = 0; e < nwv(c); ++e) seq[n++] = -1;
  }
  return r;
}
constexpr ASeq ASEQ = make_aseq();

template <class F, class F2> DI void softmax32(const f32x16& pin, bool hi, u32x4 (&fp)[2], u32x4 (&fn)[2], F mid, F2 mid2) {
  float t[16];
#pragma unroll
  for (int i = 0; i < 16; ++i) t[i] = rbf(pin[i]);
  float tp[4], tn[4];
#pragma unroll
  for (int i = 0; i < 4; ++i) { tp[i] = hi ? -__builtin_inff() : t[12 + i]; tn[i] = hi ? __builtin_inff() : t[12 + i]; }
  float mx = fmaxf(fmaxf(t[0], t[1]), t[2]), mn = fminf(fminf(t[0], t[1]), t[2]);
#pragma unroll
  for (int i = 3; i < 11; i += 2) { mx = fmaxf(fmaxf(mx, t[i]), t[i + 1]); mn = fminf(fminf(mn, t[i]), t[i + 1]); }
  mx = fmaxf(fmaxf(mx, t[11]), tp[0]); mx = fmaxf(fmaxf(mx, tp[1]), tp[2]); mx = fmaxf(mx, tp[3]);
  mn = fminf(fminf(mn, t[11]), tn[0]); mn = fminf(fminf(mn, tn[1]), tn[2]); mn = fminf(mn, tn[3]);
  { float a, b; swap32(mx, a, b); mx = fmaxf(a, b); swap32(mn, a, b); mn = fminf(a, b); }
  mid();
  const f32x2 L2 = {LOG2E, LOG2E}, NL2 = {-LOG2E, -LOG2E};
  const f32x2 mxs = (f32x2){mx, mx} * NL2, mns = (f32x2){mn, mn} * L2;
  float ep[16], en[16];
#pragma unroll
  for (int i = 0; i < 16; i += 2) {
    const f32x2 a = i < 12 ? (f32x2){t[i], t[i + 1]} : (f32x2){tp[i - 12], tp[i - 11]};
    const f32x2 b = i < 12 ? (f32x2){t[i], t[i + 1]} : (f32x2){tn[i - 12], tn[i - 11]};
    const f32x2 xp = __builtin_elementwise_fma(a, L2, mxs), xn = __builtin_elementwise_fma(b, NL2, mns);
    ep[i] = exp2f_(xp[0]); ep[i + 1] = exp2f_(xp[1]); en[i] = exp2f_(xn[0]); en[i + 1] = exp2f_(xn[1]);
  }
  mid2();
  f32x2 sp2 = {ep[0], ep[1]}, sn2 = {en[0], en[1]};
#pragma unroll
  for (int i = 2; i < 16; i += 2) { sp2 += (f32x2){ep[i], ep[i + 1]}; sn2 += (f32x2){en[i], en[i + 1]}; }
  float sp = sp2[0] + sp2[1], sn = sn2[0] + sn2[1];
  { float a, b; swap32(sp, a, b); sp = a + b; swap32(sn, a, b); sn = a + b; }
  const float ip = rcpf_(sp), in = rcpf_(sn);
#pragma unroll
  for (int s = 0; s < 2; ++s)
#pragma unroll
    for (int k = 0; k < 4; ++k) {
      const f32x2 a = (f32x2){ep[8 * s + 2 * k], ep[8 * s + 2 * k + 1]} * (f32x2){ip, ip};
      const f32x2 b = (f32x2){en[8 * s + 2 * k], en[8 * s + 2 * k + 1]} * (f32x2){in, in};
      fp[s][k] = pk_bf16(a[0], a[1]);
      fn[s][k] = pk_bf16(b[0], b[1]);
    }
}
DI u32x4 pack8(const f32x16& a, int s) { return (u32x4){pk_bf16(a[8 * s], a[8 * s + 1]), pk_bf16(a[8 * s + 2], a[8 * s + 3]), pk_bf16(a[8 * s + 4], a[8 * s + 5]), pk_bf16(a[8 * s + 6], a[8 * s + 7])}; }

DI void roleA(const Params& p, int xcd, int o, LAS char* lds, rsrc_t rsws) {
  const int tid = threadIdx.x, lane = tid & 63, w = __builtin_amdgcn_readfirstlane(tid >> 6);
  const int h = 8 * (xcd & 1) + (int)((((xcd & 1) ? 0x75310642u : 0x64207531u) >> (4 * (o & 7))) & 7u), rr = 4 * (xcd >> 1) + (o >> 3);
  const int wsh = h == 15 ? 8 : 0;
  const int l32 = lane & 31, hh = lane >> 5, l16 = lane & 15, g = lane >> 4;
  const bool hi = lane >= 32;
#define APRIO 1
#define AHI 3
#define AINIT APRIO
  SETPRIO(AINIT);
  const u32 ldsb = (u32)(uintptr_t)lds;
  const rsrc_t rsW = mkrs(p.wqkv, D * 3 * D * 2), rsP = mkrs(p.wphi, H * DH * DH * 2), rsX = mkrs(p.x, S * D * 2);
  const u32 wvoff = ((lane >> 2) * (3 * D) + h * DH) * 2 + (lane & 3) * 16 - wsh;
  const int r0 = rr * 128 + w * 32;
  int c0 = 2 * (rr & 3) + (h & 7); c0 = c0 >= 7 ? c0 - 7 : c0; c0 = c0 >= 7 ? c0 - 7 : c0;
  u32x4 xr[7][4];
  const u32 xvoff = (r0 + (lane >> 3)) * (D * 2) + 16 * (lane & 7);
  auto chunk_of = [&](int i) { int c = c0 + i; return c >= 7 ? c - 7 : c; };
  auto wpiece = [&](int dst, int part, int i) { dma16(ldsb + dst + w * 1024, wvoff, rsW, (chunk_of(i) * 64 + w * 16) * (3 * D * 2) + part * (D * 2)); };
  auto issue = [&](int i) {
    const u32 c = chunk_of(i);
    wpiece(LA_WK + i * 4096, 1, i);
#pragma unroll
    for (int k = 0; k < 4; ++k) ald16(xr[i][k], rsX, xvoff, c * 128 + k * 8 * D * 2);
  };
  u32 ev;
  asm volatile("buffer_load_dword %0, off, %1, 0 sc1" : "=v"(ev) : "s"(rsws) : "memory");
  static_assert(WS_EPOCH == 0, "");
  dma16(ldsb + LA_WP + (w & 1) * 1024, (h * (DH * DH) + ((w & 1) * 16 + (lane >> 2)) * DH) * 2 + (lane & 3) * 16, rsP, 0);
  const u32 ob = WS_OF + (u32)(r0 >> 5) * OFS + (u32)h * OFH + lane * 16;
  const u32 g2v = tid < 16 ? WS_G2 + (u32)h * 4096 + (u32)(rr >> 2) * 1024 + (u32)(((rr & 1) * 32 + ((rr >> 1) & 1) * 16 + tid) * 16) : OOB;
#pragma unroll
  for (int i = 0; i < LOOKA; ++i) issue(i);
  if (AINIT != APRIO) SETPRIO(APRIO);
  LAS char* xs = lds + LA_XS + w * 4096;
  const int r3 = lane >> 3, ch8 = lane & 7;
  LAS char* wa0 = xs + r3 * 128 + ((ch8 ^ (r3 >> 1)) << 4);
  LAS char* wa1 = xs + r3 * 128 + ((ch8 ^ (r3 >> 1) ^ 4) << 4);
  LAS char* ra[4];
#pragma unroll
  for (int j = 0; j < 4; ++j) ra[j] = xs + l32 * 128 + (((2 * j + hh) ^ ((l32 >> 1) & 7)) << 4);
  LAS char* wb = lds + (8 * hh + ((lane & 15) >> 2)) * 64 + 32 * ((lane >> 4) & 1) + 8 * (lane & 3) + wsh;
  if (APIN) asm volatile("" : "+v"(wa0), "+v"(wa1), "+v"(ra[0]), "+v"(ra[1]), "+v"(ra[2]), "+v"(ra[3]), "+v"(wb));
  u32x4 xf[28];
  f32x16 ak, ak2;
#pragma unroll
  for (int i = 0; i < 16; ++i) { ak[i] = 0.f; ak2[i] = 0.f; }
  u32x4 wk[2][4];
  u32 tag = 0;
#pragma unroll
  for (int c = 0; c < 8; ++c) {
    if (c < 7) {
      if (c == 0) waitv4<ASEQ.w[0]>(xr[0][0], xr[0][1], xr[0][2], xr[0][3]);
      else if (c == 1) waitv4<ASEQ.w[1]>(xr[1][0], xr[1][1], xr[1][2], xr[1][3]);
      else if (c == 2) waitv4<ASEQ.w[2]>(xr[2][0], xr[2][1], xr[2][2], xr[2][3]);
      else if (c == 3) waitv4<ASEQ.w[3]>(xr[3][0], xr[3][1], xr[3][2], xr[3][3]);
      else if (c == 4) waitv4<ASEQ.w[4]>(xr[4][0], xr[4][1], xr[4][2], xr[4][3]);
      else if (c == 5) waitv4<ASEQ.w[5]>(xr[5][0], xr[5][1], xr[5][2], xr[5][3]);
      else waitv4<ASEQ.w[6]>(xr[6][0], xr[6][1], xr[6][2], xr[6][3]);
      if (c == 0) { asm volatile("" : "+v"(ev)); tag = (u32)__builtin_amdgcn_readfirstlane(ev) + 1u; }
      __builtin_amdgcn_s_barrier();
      asm volatile("" ::: "memory");
      SETPRIO(AHI);
      *(LAS u32x4*)(wa0) = xr[c][0]; *(LAS u32x4*)(wa1 + 1024) = xr[c][1]; *(LAS u32x4*)(wa0 + 2048) = xr[c][2]; *(LAS u32x4*)(wa1 + 3072) = xr[c][3];
#pragma unroll
      for (int j = 0; j < 4; ++j) {
        const int ks = 4 * c + j;
        xf[ks] = *(LAS u32x4*)(ra[j]);
        wk[c & 1][j] = cat(tr_read(wb + LA_WK + ks * 1024), tr_read(wb + LA_WK + ks * 1024 + 256));
      }
    }
    if (c > 0) {
#pragma unroll
      for (int j = 0; j < 4; ++j) {
        const int ks = 4 * (c - 1) + j;
        if (ACC2K && (j & 1)) ak2 = mfma32(wk[(c - 1) & 1][j], xf[ks], ak2); else ak = mfma32(wk[(c - 1) & 1][j], xf[ks], ak);
      }
    }
    if (c < 7) {
#pragma unroll
      for (int j = 0; j < 4; ++j) { __builtin_amdgcn_sched_group_barrier(0x300, 4, 0); if (c > 0) __builtin_amdgcn_sched_group_barrier(0x8, 1, 0); }
    }
    if (c > 0) { asm volatile("" : "+v"(ak)); if (ACC2K) asm volatile("" : "+v"(ak2)); }
    __builtin_amdgcn_sched_barrier(0);
    if (c < 7) SETPRIO(APRIO);
    if (c + LOOKA < 7) issue(c + LOOKA);
    if (c == 0) {
      st16_sc1(rsws, g2v, (u32x4){SENT, SENT, SENT, SENT});
    }
#pragma unroll
    for (int i = nwv0(c); i < nwv0(c) + nwv(c); ++i) wpiece(LA_WV + i * 4096, 2, i);
  }
  if (ACC2K) ak += ak2;
  u32x4 wf[2];
  {
    LAS char* pb = lds + LA_WP + (4 * hh + ((lane & 15) >> 2)) * 64 + 32 * ((lane >> 4) & 1) + 8 * (lane & 3);
#pragma unroll
    for (int s = 0; s < 2; ++s) wf[s] = cat(tr_read(pb + s * 1024), tr_read(pb + s * 1024 + 512));
  }
  if (WQPOS == 0) {
    bar();
#pragma unroll
    for (int i = 0; i < 7; ++i) wpiece(wq_slot(i), 0, i);
  }
  f32x16 z16;
#pragma unroll
  for (int i = 0; i < 16; ++i) z16[i] = 0.f;
  {
    u32x4 k0 = pack8(ak, 0), k1 = pack8(ak, 1);
    if (hi) { k1[2] = 0; k1[3] = 0; }
    f32x16 pk = mfma32(wf[0], k0, z16);
    pk = mfma32(wf[1], k1, pk);
    const int sw = (l32 >> 1) & 7;
    u32x4 fp[2], fn[2];
    f32x16 av = z16, av2 = z16;
    auto vgemm = [&](int g0, int g1) {
      u32x4 wv[7][4];
#pragma unroll
      for (int grp = g0; grp < g1; ++grp)
#pragma unroll
        for (int j = 0; j < 4; ++j) { LAS char* wp = wb + LA_WV + grp * 4096 + j * 1024; wv[grp][j] = cat(tr_read(wp), tr_read(wp + 256)); }
#pragma unroll
      for (int grp = g0; grp < g1; ++grp)
#pragma unroll
        for (int j = 0; j < 4; ++j) { if (ACC2 && (j & 1)) av2 = mfma32(wv[grp][j], xf[4 * grp + j], av2); else av = mfma32(wv[grp][j], xf[4 * grp + j], av); }
    };
    auto vsched = [&](int ng, int nv) {
      __builtin_amdgcn_sched_group_barrier(0x100, 2 * VLA, 0);
      __builtin_amdgcn_sched_group_barrier(0x2, 6, 0);
#pragma unroll
      for (int m = 0; m < 4 * ng; ++m) {
        __builtin_amdgcn_sched_group_barrier(0x8, 1, 0);
        if (m < 4 * ng - VLA) __builtin_amdgcn_sched_group_barrier(0x100, 2, 0);
        if (nv == 2) __builtin_amdgcn_sched_group_barrier(0x2, 2, 0); else if (nv == 3) __builtin_amdgcn_sched_group_barrier(0x202, 3, 0); else if (nv == 4) __builtin_amdgcn_sched_group_barrier(0x202, 4, 0); else __builtin_amdgcn_sched_group_barrier(0x2, 1, 0);
      }
      __builtin_amdgcn_sched_barrier(0);
    };
    auto vstart = [&]() {
      __builtin_amdgcn_sched_barrier(0);
      waitvm<WQPOS == 0 ? 7 : 0>();
      bar();
      if (WQPOS == 1) {
#pragma unroll
        for (int i = 0; i < 7; ++i) wpiece(wq_slot(i), 0, i);
      }
      __builtin_amdgcn_sched_barrier(0);
      vgemm(0, 7);
    };
    auto vend = [&](int nv) {
      vsched(7, nv);
      bar();
    };
    softmax32(pk, hi, fp, fn, [&]() { if (VMODE == 0) vstart(); }, [&]() { if (VMODE == 0) vend(VNV); else vstart(); });
#pragma unroll
    for (int s = 0; s < 2; ++s) {
      *(LAS u32x2*)(xs + l32 * 64 + 8 * ((4 * s + hh) ^ sw)) = (u32x2){fp[s][0], fp[s][1]};
      *(LAS u32x2*)(xs + l32 * 64 + 8 * ((4 * s + 2 + hh) ^ sw)) = (u32x2){fp[s][2], fp[s][3]};
      *(LAS u32x2*)(xs + 2048 + l32 * 64 + 8 * ((4 * s + hh) ^ sw)) = (u32x2){fn[s][0], fn[s][1]};
      *(LAS u32x2*)(xs + 2048 + l32 * 64 + 8 * ((4 * s + 2 + hh) ^ sw)) = (u32x2){fn[s][2], fn[s][3]};
    }
    if (VMODE == 1) vend(VNV1);
    if (ACC2) av += av2;
    LAS char* vt = lds + LA_V + w * 2048 + l32 * 64;
    {
      u32x4 v0 = pack8(av, 0), v1 = pack8(av, 1);
      if (hi) { v1[2] = 0x00003f80u; v1[3] = 0; }
      *(LAS u32x2*)(vt + 8 * ((hh) ^ sw)) = (u32x2){v0[0], v0[1]};
      *(LAS u32x2*)(vt + 8 * ((2 + hh) ^ sw)) = (u32x2){v0[2], v0[3]};
      *(LAS u32x2*)(vt + 8 * ((4 + hh) ^ sw)) = (u32x2){v1[0], v1[1]};
      *(LAS u32x2*)(vt + 8 * ((6 + hh) ^ sw)) = (u32x2){v1[2], v1[3]};
    }
  }
  bar();
  {
    const int q = l16 >> 2, pp = lane & 3;
    const int ra_ = 8 * g + q, rb_ = ra_ + 4;
    const int sa = (ra_ >> 1) & 7, sb = (rb_ >> 1) & 7;
    LAS char* pa = lds + LA_XS + (w >> 1) * 2048;
    const int ua = 4 * (w & 1) + pp;
    f32x4 kv0 = {0, 0, 0, 0}, kv1 = {0, 0, 0, 0};
#pragma unroll
    for (int kt = 0; kt < 4; ++kt) {
      const u32x4 A = cat(tr_read(pa + kt * 4096 + ra_ * 64 + 8 * (ua ^ sa)), tr_read(pa + kt * 4096 + rb_ * 64 + 8 * (ua ^ sb)));
      LAS char* vb = lds + LA_V + kt * 2048;
      const u32x4 B0 = cat(tr_read(vb + ra_ * 64 + 8 * (pp ^ sa)), tr_read(vb + rb_ * 64 + 8 * (pp ^ sb)));
      const u32x4 B1 = cat(tr_read(vb + ra_ * 64 + 8 * ((4 + pp) ^ sa)), tr_read(vb + rb_ * 64 + 8 * ((4 + pp) ^ sb)));
      kv0 = mfma16(A, B0, kv0);
      kv1 = mfma16(A, B1, kv1);
    }
    const u32 base = WS_G1 + (u32)((h * 16 + rr) * 8 + 2 * w) * 1024 + lane * 16;
    const u32 t8 = tag & 0xffu;
    st16_sc1(rsws, base, (u32x4){(f2u(kv0[0]) & ~0xffu) | t8, (f2u(kv0[1]) & ~0xffu) | t8, (f2u(kv0[2]) & ~0xffu) | t8, (f2u(kv0[3]) & ~0xffu) | t8});
    st16_sc1(rsws, base + 1024, (u32x4){(f2u(kv1[0]) & ~0xffu) | t8, (f2u(kv1[1]) & ~0xffu) | t8, (f2u(kv1[2]) & ~0xffu) | t8, (f2u(kv1[3]) & ~0xffu) | t8});
  }
  u32x4 pv[2];
  const u32 g1v = (u32)(lane >> 5) * 8192 + (u32)((lane >> 4) & 1) * 512 + (u32)l16 * 16;
  const u32 g1s = WS_G1 + (u32)(h * 16 + 4 * w) * 8192 + (u32)(rr >> 1) * 1024 + (u32)(rr & 1) * 256;
  if (PVPOS == 0) {
#pragma unroll
    for (int s = 0; s < 2; ++s) ald16s(pv[s], rsws, g1v, g1s + s * 16384);
  }
  static_assert(PVPOS != 0, "");
  const bool doF = o < 28;
  const int frg = 4 * xcd + o / 7, fcg = o % 7;
  const int C0 = 64 * fcg;
  const rsrc_t rsO = mkrs(p.wo, doF ? D * D * 2 : 0);
  u32 wovoff = (u32)(((lane >> 3) * D + C0) * 2 + 16 * (((((lane & 7) >> 1) ^ ((lane >> 4) & 3)) << 1) | (lane & 1)));
  if (APIN) asm volatile("" : "+v"(wovoff));
  waitvm<2>();
  __builtin_amdgcn_s_barrier();
  asm volatile("" ::: "memory");
  u32 pf0 = 0, pf1 = 0;
  if (WOL2) {
    const u32 pv_ = (u32)((112 * w + lane) * D + C0) * 2;
    asm volatile("buffer_load_dword %0, %1, %2, 0 offen" : "=v"(pf0) : "v"(pv_), "s"(rsO) : "memory");
    asm volatile("buffer_load_dword %0, %1, %2, 0 offen" : "=v"(pf1) : "v"(lane < 48 ? pv_ + 64 * D * 2 : OOB), "s"(rsO) : "memory");
  }
  f32x16 aq = z16, aq2 = z16;
  {
    u32x4 wq[3][4];
    auto rdq = [&](int grp) {
#pragma unroll
      for (int j = 0; j < 4; ++j) { LAS char* wp = wb + wq_slot(grp) + j * 1024; wq[grp % 3][j] = cat(tr_read(wp), tr_read(wp + 256)); }
    };
    rdq(0);
    if (QLA == 2) rdq(1);
    __builtin_amdgcn_sched_barrier(0);
#pragma unroll
    for (int grp = 0; grp < 7; ++grp) {
      if (grp + QLA < 7) rdq(grp + QLA);
#pragma unroll
      for (int j = 0; j < 4; ++j) aq = mfma32(wq[grp % 3][j], xf[4 * grp + j], aq);
      if (grp + QLA < 7) {
#pragma unroll
        for (int j = 0; j < 4; ++j) { __builtin_amdgcn_sched_group_barrier(0x8, 1, 0); __builtin_amdgcn_sched_group_barrier(0x100, 2, 0); }
      }
      __builtin_amdgcn_sched_barrier(0);
      const int blk = 28 + w + 4 * grp;
      if (WOV == 0 || WOV == 2) dma16n(ldsb + LA_WO + blk * 1024, wovoff, rsO, blk * 8 * D * 2);
    }
  }
  if (ACC2) aq += aq2;
  u32x4 wr[14];
  if (WOV == 5) {
    asm volatile("" : "+v"(aq));
#pragma unroll
    for (int i = 0; i < 14; ++i) ald16(wr[i], rsO, wovoff, (u32)((w + 4 * i) * 8 * D * 2));
  }
  auto pvld = [&]() {
#pragma unroll
    for (int s = 0; s < 2; ++s) ald16s(pv[s], rsws, g1v, g1s + s * 16384);
  };
  if (PVPOS == 1) pvld();
  u32x4 fp[2], fn[2];
  auto smq = [&]() {
    u32x4 q0 = pack8(aq, 0), q1 = pack8(aq, 1);
    if (hi) { q1[2] = 0; q1[3] = 0; }
    f32x16 pq = mfma32(wf[0], q0, z16);
    pq = mfma32(wf[1], q1, pq);
    softmax32(pq, hi, fp, fn, [&]() { if (PVPOS == 3 && SMQ == 1) { __builtin_amdgcn_sched_barrier(0); pvld(); __builtin_amdgcn_sched_barrier(0); } }, []() {});
    tie(fp[0]); tie(fp[1]); tie(fn[0]); tie(fn[1]);
  };
  if (SMQ == 1) smq();
  if (PVPOS == 2) pvld();
  LAS char* red = lds + LA_XS + 4096;
  LAS char* kvi = lds + LA_XS + 8192;
  {
    asm volatile("s_waitcnt vmcnt(%4)" : "+v"(pv[0]), "+v"(pv[1]), "+v"(pf0), "+v"(pf1) : "n"(WOV == 0 && PVPOS == 0 ? 7 : 0) : "memory");
    if (WOV == 5) {
#pragma unroll
      for (int i = 0; i < 14; ++i) tie(wr[i]);
    }
    auto pv_ok = [&]() {
      u32 m = (pv[0][0] ^ tag) | (pv[0][1] ^ tag) | (pv[0][2] ^ tag) | (pv[0][3] ^ tag);
      m |= (pv[1][0] ^ tag) | (pv[1][1] ^ tag) | (pv[1][2] ^ tag) | (pv[1][3] ^ tag);
      return __all((m & 0xffu) == 0); };
    if (__builtin_expect(!pv_ok(), 0)) {
      u32 spins = 0;
#pragma unroll 1
      do {
        if (spin_fail(spins, rsws)) break;
#pragma unroll
        for (int s = 0; s < 2; ++s) pv[s] = ld16_sc1(rsws, g1s + s * 16384 + g1v);
      } while (!pv_ok());
    }
    f32x4 a;
#pragma unroll
    for (int r = 0; r < 4; ++r) { const u32 x0 = pv[0][r], x1 = pv[1][r]; a[r] = u2f(x0 & ~0xffu) + u2f(x1 & ~0xffu); }
    *(LAS f32x4*)(red + w * 1024 + lane * 16) = a;
    bar();
    if (w == 0) {
      const f32x4 r1 = *(LAS f32x4*)(red + 1024 + lane * 16), r2 = *(LAS f32x4*)(red + 2048 + lane * 16), r3 = *(LAS f32x4*)(red + 3072 + lane * 16);
      const f32x4 t = (a + r1) + (r2 + r3);
      const auto s02 = __builtin_amdgcn_permlane32_swap(f2u(t[0]), f2u(t[2]), false, false);
      const auto s13 = __builtin_amdgcn_permlane32_swap(f2u(t[1]), f2u(t[3]), false, false);
      const u32 x0 = s02[0], y0 = s02[1], x1 = s13[0], y1 = s13[1];
      const u32 dd = sane(pk_bf16(u2f(x0) + u2f(y0), u2f(x1) + u2f(y1)));
      const auto q16 = __builtin_amdgcn_permlane16_swap(dd, dd, false, false);
      const u32 e0 = q16[0], e1 = q16[1];
      const auto h0 = __builtin_amdgcn_permlane32_swap(e0, e0, false, false), h1 = __builtin_amdgcn_permlane32_swap(e1, e1, false, false);
      const u32 a1 = h0[1], b1 = h1[1];
      st16_sc1(rsws, g2v, (u32x4){e0, a1, e1, b1});
    }
  }
  const u32 goff = WS_G2 + (u32)h * 4096 + (u32)w * 1024 + (u32)lane * 16;
  u32x4 kt;
  u32 flg = tag;
  if (KTPOS == 0) ald16s(kt, rsws, (u32)lane * 16, WS_G2 + (u32)h * 4096 + (u32)w * 1024);
  if (WOV == 5) {
#pragma unroll
    for (int i = 0; i < 14; ++i) *(LAS u32x4*)(lds + LA_WO + (w + 4 * i) * 1024 + lane * 16) = wr[i];
  }
  if (WOV == 1) {
#pragma unroll
    for (int i = 0; i < 14; ++i) { const int blk = w + 4 * ((i + 7) % 14); dma16(ldsb + LA_WO + blk * 1024, wovoff, rsO, blk * 8 * D * 2); }
  }
  if (tid == 64) st4_sc1(rsws, WS_SEEN + 1024 + (u32)(rr * 16 + h) * 4, tag);
  if (SMQ == 0) smq();
  if (WOV == 0) {
#pragma unroll
  for (int i = 0; i < 7; ++i) { const int blk = w + 4 * i; dma16(ldsb + LA_WO + blk * 1024, wovoff, rsO, blk * 8 * D * 2); }
  }
  if (KSLEEP) __builtin_amdgcn_s_sleep(KSLEEP);
  if (KTPOS == 1) ald16s(kt, rsws, (u32)lane * 16, WS_G2 + (u32)h * 4096 + (u32)w * 1024);
  {
    waitv<KTPOS == 1 || WOV >= 2 ? 0 : WOV == 0 ? 7 : 14>(kt);
    if (__any(max4(kt) == SENT)) {
      u32 spins = 0;
#pragma unroll 1
      do {
        if (spin_fail(spins, rsws)) break;
        kt = ld16_sc1(rsws, goff);
      } while (__any(max4(kt) == SENT));
    }
    *(LAS u32x4*)(kvi + w * 1024 + lane * 16) = kt;
    waitvm<0>();
    bar();
    u32x4 kvf[4];
#pragma unroll
    for (int f = 0; f < 4; ++f) kvf[f] = *(LAS u32x4*)(kvi + f * 1024 + lane * 16);
    f32x16 n = mfma32(kvf[0], fp[0], z16);
    f32x16 n2 = mfma32(kvf[2], fn[0], z16);
    n = mfma32(kvf[1], fp[1], n);
    n2 = mfma32(kvf[3], fn[1], n2);
    n += n2;
    float a, b; swap32(n[12], a, b);
    const float id = rcpf_(rbf(rbf(b) + 1e-6f));
    float ov[16];
#pragma unroll
    for (int i = 0; i < 16; ++i) ov[i] = rbf(n[i]) * id;
    const u32 th = tag << 16;
    float x0, x1; swap32(u2f(pk_bf16(ov[14], ov[15])), x0, x1);
    u32x4 of[2];
    of[0] = (u32x4){pk_bf16(ov[0], ov[1]), pk_bf16(ov[2], ov[3]), pk_bf16(ov[4], ov[5]), (pk_bf16(ov[6], 0.f) & 0xffffu) | th};
    of[1] = (u32x4){pk_bf16(ov[7], ov[8]), pk_bf16(ov[9], ov[10]), hi ? f2u(x0) : pk_bf16(ov[12], ov[13]), (pk_bf16(ov[11], 0.f) & 0xffffu) | th};
    st16_sc1(rsws, ob, of[0]); st16_sc1(rsws, ob + 1024, of[1]);
  }
  if (WOV == 2) {
#pragma unroll
    for (int i = 0; i < 7; ++i) { const int blk = w + 4 * i; dma16(ldsb + LA_WO + blk * 1024, wovoff, rsO, blk * 8 * D * 2); }
  }
  if (WOV == 3) {
#pragma unroll
    for (int i = 0; i < 14; ++i) { const int blk = w + 4 * i; dma16(ldsb + LA_WO + blk * 1024, wovoff, rsO, blk * 8 * D * 2); }
  }
  if (doF) {
    const int R0 = frg * 64 + 16 * w;
    const int rt16 = R0 >> 4;
    const int l = R0 + l16;
    constexpr int ORD[16] = {WOV == 2 ? 8 : 0, WOV == 2 ? 10 : 1, WOV == 2 ? 12 : 3, WOV == 2 ? 14 : 5, WOV == 2 ? 15 : 7, WOV == 2 ? 9 : 8, WOV == 2 ? 11 : 10, WOV == 2 ? 13 : 12,
                             WOV == 2 ? 0 : 14, WOV == 2 ? 1 : 15, WOV == 2 ? 3 : 2, WOV == 2 ? 5 : 4, WOV == 2 ? 7 : 6, WOV == 2 ? 2 : 9, WOV == 2 ? 4 : 11, WOV == 2 ? 6 : 13};
    const int q = l16 >> 2, pp = lane & 3;
    LAS char* wob1[4];
    LAS char* wob2[4];
    {
      const int h4 = 4 * (g & 1), h2 = 2 * (g & 1);
      const int r1 = g < 2 ? h4 + q : (q == 0 ? 11 + h4 : 15 + h4 + q);
      const int r2 = g < 2 ? 8 + h4 + q : (q < 2 ? 24 + h2 + q : q == 2 ? 19 + h4 : 20 + h2);
#pragma unroll
      for (int ct = 0; ct < 4; ++ct) {
        wob1[ct] = lds + LA_WO + r1 * 128 + ((ct ^ ((r1 >> 1) & 3)) << 5) + 8 * pp;
        wob2[ct] = lds + LA_WO + r2 * 128 + ((ct ^ ((r2 >> 1) & 3)) << 5) + 8 * pp;
        asm volatile("" : "+v"(wob1[ct]), "+v"(wob2[ct]));
      }
    }
    auto wfrag = [&](int hd, int ct) { return cat(tr_read(wob1[ct ^ (2 * (hd & 1))] + 3584 * hd), tr_read(wob2[ct ^ (2 * (hd & 1))] + 3584 * hd)); };
    u32x4 wA[4][4], wB[4][4];
#define RDW(dst, gr) do { _Pragma("unroll") for (int k = 0; k < 4; ++k) _Pragma("unroll") for (int ct = 0; ct < 4; ++ct) dst[k][ct] = wfrag(ORD[4 * (gr) + k], ct); } while (0)
    if (WOV != 3) { RDW(wA, 0); RDW(wB, 1); }
    u32 ovoff = (u32)(rt16 >> 1) * OFS + (u32)((g >> 1) * 64 + l16 + 16 * (rt16 & 1) + 32 * (g & 1)) * 16;
    u32 bvoff = WS_YB + (u32)(C0 / 2 + 2 * g) * YBS + (u32)l * 4;
    u32 gvo = (u32)((frg * 7 + g) * 64 + (l & 63)) * 8;
    u32 outo = (u32)(l * D + C0 + 4 * g) * 2;
    u32 floff = WS_SEEN + (u32)(C0 / 2 + (lane & 31)) * 4;
    if (FPIN) asm volatile("" : "+v"(ovoff), "+v"(bvoff), "+v"(gvo), "+v"(outo));
    const rsrc_t rsOut = mkrs(p.out, S * D * 2);
    if ((0x2a54u >> h) & 1) { if (FSLEEPS) __builtin_amdgcn_s_sleep(FSLEEPS); } else { if (FSLEEP) __builtin_amdgcn_s_sleep(FSLEEP); }
    u32x4 of[16];
    u32x2 gv, gw;
    u32 bv[8];
    const u32 gwo = g < 3 ? gvo + 4 * 512 : OOB;
    asm volatile("buffer_load_dword %0, %1, %2, 0 offen sc1" : "=v"(flg) : "v"(floff), "s"(rsws) : "memory");
#pragma unroll
    for (int k = 0; k < 8; ++k) FLD(of[k], rsws, ovoff, WS_OF + ORD[k] * OFH);
    if (FSLEEP2) { if ((0x2a54u >> h) & 1) { if (FSLEEP2S) __builtin_amdgcn_s_sleep(FSLEEP2S); } else __builtin_amdgcn_s_sleep(FSLEEP2); }
#pragma unroll
    for (int k = 8; k < 16; ++k) FLD(of[k], rsws, ovoff, WS_OF + ORD[k] * OFH);
    if (WOV == 3) {
      waitvm<17>();
      __builtin_amdgcn_s_barrier();
      asm volatile("" ::: "memory");
      RDW(wA, 0); RDW(wB, 1);
      __builtin_amdgcn_sched_barrier(0);
    }
    const u32 th = tag << 16;
    auto stale = [&](const u32x4& v) { return __any((v[3] ^ th) >= 0x10000u); };
    auto ywait = [&]() {
      if (__builtin_expect(!__all(flg == tag), 0)) {
        u32 spins = 0;
#pragma unroll 1
        do {
          if (spin_fail(spins, rsws)) break;
          flg = ld4_sc1(rsws, floff);
        } while (!__all(flg == tag));
      }
    };
#define YLOAD(C) do { \
      asm volatile("buffer_load_dwordx2 %0, %1, %2, %3 offen sc1" : C(gv) : "v"(gvo), "s"(rsws), "s"(WS_G) : "memory"); \
      asm volatile("buffer_load_dwordx2 %0, %1, %2, %3 offen sc1" : C(gw) : "v"(gwo), "s"(rsws), "s"(WS_G) : "memory"); \
      _Pragma("unroll") for (int i = 0; i < 8; ++i) asm volatile("buffer_load_dword %0, %1, %2, %3 offen sc1" : C(bv[i]) : "v"(bvoff), "s"(rsws), "s"((u32)((i >> 1) * 8 + (i & 1)) * YBS) : "memory"); \
    } while (0)
    static_assert(YPOS >= 0, "the flag load is waited for with the first group");
    f32x4 acc[4];
#pragma unroll
    for (int ct = 0; ct < 4; ++ct) acc[ct] = (f32x4){0, 0, 0, 0};
#define FMMA(W, gr) do { _Pragma("unroll") for (int k = 0; k < 4; ++k) { of[4 * (gr) + k][3] &= 0xffffu; _Pragma("unroll") for (int ct = 0; ct < 4; ++ct) acc[ct] = mfma16(W[k][ct], of[4 * (gr) + k], acc[ct]); } } while (0)
#define RELOAD(k) asm volatile("buffer_load_dwordx4 %0, %1, %2, %3 offen sc1" : "+v"(of[k]) : "v"(ovoff), "s"(rsws), "s"(WS_OF + ORD[k] * OFH) : "memory")
#define FSCHED() do { if (FIL) { __builtin_amdgcn_sched_group_barrier(0x2, 4, 0); _Pragma("unroll") for (int i_ = 0; i_ < 16; ++i_) { __builtin_amdgcn_sched_group_barrier(0x8, 1, 0); __builtin_amdgcn_sched_group_barrier(0x100, 2, 0); } } } while (0)
    u32 bad = 0, done = 0;
#define FCHK(gr) do { \
      waitv4<12 - 4 * (gr) + ((gr) > YPOS ? 10 : 0)>(of[4 * (gr)], of[4 * (gr) + 1], of[4 * (gr) + 2], of[4 * (gr) + 3]); \
      if (FCOMB) { \
        if (__builtin_expect(__any((((of[4 * (gr)][3] ^ th) | (of[4 * (gr) + 1][3] ^ th)) | ((of[4 * (gr) + 2][3] ^ th) | (of[4 * (gr) + 3][3] ^ th))) >= 0x10000u), 0)) { \
          bad |= 0xfu << (4 * (gr)); _Pragma("unroll") for (int k = 4 * (gr); k < 4 * (gr) + 4; ++k) RELOAD(k); } \
      } else { \
        _Pragma("unroll") for (int k = 4 * (gr); k < 4 * (gr) + 4; ++k) if (__builtin_expect(stale(of[k]), 0)) { bad |= 1u << k; RELOAD(k); } \
      } \
    } while (0)
    FCHK(0);
    asm volatile("" : "+v"(flg));
    if (WOV == 2) { __builtin_amdgcn_s_barrier(); asm volatile("" ::: "memory"); }
    if (bad == 0) { FMMA(wA, 0); RDW(wA, 2); FSCHED(); done = 1; }
    if (YPOS == 0) { ywait(); YLOAD("=&v"); }
    FCHK(1);
    if (bad == 0) { FMMA(wB, 1); RDW(wB, 3); FSCHED(); done = 2; }
    if (YPOS == 1) { ywait(); YLOAD("=&v"); }
    FCHK(2);
    if (bad == 0) { FMMA(wA, 2); done = 3; }
    if (YPOS == 2) { ywait(); YLOAD("=&v"); }
    FCHK(3);
    if (bad == 0) { FMMA(wB, 3); done = 4; }
#define FWAITALL() asm volatile("s_waitcnt vmcnt(0)" : "+v"(of[0]), "+v"(of[1]), "+v"(of[2]), "+v"(of[3]), "+v"(of[4]), "+v"(of[5]), "+v"(of[6]), "+v"(of[7]), \
                     "+v"(of[8]), "+v"(of[9]), "+v"(of[10]), "+v"(of[11]), "+v"(of[12]), "+v"(of[13]), "+v"(of[14]), "+v"(of[15]), \
                     "+v"(gv), "+v"(gw), "+v"(bv[0]), "+v"(bv[1]), "+v"(bv[2]), "+v"(bv[3]), "+v"(bv[4]), "+v"(bv[5]), "+v"(bv[6]), "+v"(bv[7]) :: "memory")
    auto ybad = [&]() {
      const u32 m = max(max(max(bv[0], bv[1]), max(bv[2], bv[3])), max(max(bv[4], bv[5]), max(bv[6], bv[7])));
      return __any((gv[1] != tag) | (g < 3 && gw[1] != tag) | (m == SENT)); };
    asm volatile("s_waitcnt vmcnt(0)" : "+v"(gv), "+v"(gw), "+v"(bv[0]), "+v"(bv[1]), "+v"(bv[2]), "+v"(bv[3]), "+v"(bv[4]), "+v"(bv[5]), "+v"(bv[6]), "+v"(bv[7]) :: "memory");
    u32 spins = 0;
    if (__builtin_expect(bad != 0, 0)) {
#pragma unroll 1
      for (;;) {
        FWAITALL();
        u32 nb = 0;
#pragma unroll
        for (int k = 0; k < 16; ++k) if ((bad & (1u << k)) && stale(of[k])) { nb |= 1u << k; RELOAD(k); }
        bad = nb;
        if (bad == 0) break;
        if (spin_fail(spins, rsws)) { asm volatile("s_waitcnt vmcnt(0)" ::: "memory"); break; }
      }
      if (done < 1) { FMMA(wA, 0); RDW(wA, 2); }
      if (done < 2) { FMMA(wB, 1); RDW(wB, 3); }
      if (done < 3) FMMA(wA, 2);
      FMMA(wB, 3);
    }
    if (__builtin_expect(ybad(), 0)) {
#pragma unroll 1
      do {
        if (spin_fail(spins, rsws)) break;
        YLOAD("=&v");
        asm volatile("s_waitcnt vmcnt(0)" : "+v"(gv), "+v"(gw), "+v"(bv[0]), "+v"(bv[1]), "+v"(bv[2]), "+v"(bv[3]), "+v"(bv[4]), "+v"(bv[5]), "+v"(bv[6]), "+v"(bv[7]) :: "memory");
      } while (ybad());
    }
    float gg;
    {
      const u32 s0 = gv[0], s1 = gw[0];
      float t = u2f(s0) + u2f(s1), a, b;
      swap16(t, a, b); t = a + b; swap32(t, a, b);
      const float lg = rbf(a + b);
      gg = rbf(rcpf_(1.f + exp2f_(-lg * LOG2E)));
    }
    const float g1 = rbf(1.f - gg);
#pragma unroll
    for (int ct = 0; ct < 4; ++ct) {
      const u32 b01 = bv[2 * ct], b23 = bv[2 * ct + 1];
      const float bs[4] = {bf2f(b01), bfhi(b01), bf2f(b23), bfhi(b23)};
      float ov[4];
#pragma unroll
      for (int r = 0; r < 4; ++r) ov[r] = rbf(gg * rbf(acc[ct][r])) + rbf(g1 * bs[r]);
      __builtin_amdgcn_raw_buffer_store_b64((u32x2){pk_bf16(ov[0], ov[1]), pk_bf16(ov[2], ov[3])}, rsOut, outo + 32 * ct, 0, 0);
    }
    asm volatile(".Lipf_end:");
  }
}

constexpr int CPS = 4160;
constexpr int CPCH = 4 * CPS;
constexpr int LB_CP = 0;
constexpr int LB_U = 2 * CPCH;
constexpr int LB_KC = LB_U + 2 * 4608;
constexpr int LB_MISC = LB_KC + 2 * 1536;
constexpr int LB_EX = LB_MISC + 256;
constexpr int LB_STG = 0;
constexpr int LB_TT = LB_EX + 8192;
constexpr int LB_KT = LB_EX;
static_assert(LB_EX + 24576 <= LDS_BYTES, "lds");
static_assert(LB_EX % 16 == 0, "lds");
static_assert(LB_KC % 16 == 0 && LB_KC + 32768 <= LDS_BYTES, "lds");
struct cpx2 { f32x2 re, im; };
DI cpx2 cmul(const cpx2& a, const cpx2& b) { cpx2 r; r.re = a.re * b.re - a.im * b.im; r.im = a.re * b.im + a.im * b.re; return r; }
DI cpx2 csqr(const cpx2& a) { cpx2 r; r.re = a.re * a.re - a.im * a.im; r.im = (a.re + a.re) * a.im; return r; }
DI cpx2 cexp_m(f32x2 al, f32x2 th, float m) {
  cpx2 r;
#pragma unroll
  for (int i = 0; i < 2; ++i) {
    const float mag = exp2f_(al[i] * m);
    const float ph = __builtin_amdgcn_fractf(th[i] * m);
    r.re[i] = mag * __builtin_amdgcn_cosf(ph); r.im[i] = mag * __builtin_amdgcn_sinf(ph);
  }
  return r;
}
DI void put_row(LAS char* stg, int i, const cpx2& v) { *(LAS u32x2*)(stg + i * 544) = (u32x2){pk_f16(v.re[0], v.im[0]), pk_f16(v.re[1], v.im[1])}; }
DI float pow2_scale(float mx, int target) {
  const int e = (int)((__builtin_bit_cast(u32, mx) >> 23) & 0xff);
  int se = 254 + target - e;
  se = se < 1 ? 1 : (se > 254 ? 254 : se);
  return __builtin_bit_cast(float, (u32)se << 23);
}
DI u32 kpos(int l) {
  const u32 r = (pk_bf16((float)l, 0.f) & 0xffffu) - 17024u;
  const u32 v = l < 128 ? (u32)l : r;
  return (l < 0 ? 641u : v) * 2u;
}

DI void roleB(const Params& p, int xcd, int o, LAS char* lds, rsrc_t rsws) {
  const int tid = threadIdx.x, lane = tid & 63, w = __builtin_amdgcn_readfirstlane(tid >> 6);
  const int l16 = lane & 15, g = lane >> 4;
  const int pair = xcd * 28 + o;
  const int d0 = 2 * pair;
  const int ch = w >> 1, half = w & 1, d = d0 + ch;
  LAS float* misc = (LAS float*)(lds + LB_MISC);
#define BK 2
#define BK0 BK
#define BKW BK
  SETPRIO(BK0);
  const u32 ev = ld4_sc1(rsws, WS_EPOCH);
  const int pi = (d * NS + half * 128 + 2 * lane) >> 1;
  const u32 are = ((const u32*)p.Are)[pi], aim = ((const u32*)p.Aim)[pi], bre = ((const u32*)p.Bre)[pi], bim = ((const u32*)p.Bim)[pi], cre = ((const u32*)p.Cre)[pi], cim = ((const u32*)p.Cim)[pi];
  const float ldt = bf2f(p.logdt[d]);
  const u32 dsk2 = ((const u32*)p.dskip)[pair];
  const int tt_ = (xcd & 1) * 28 + o;
  const int trt = 8 * (xcd >> 1) + tt_ / 7, tcg = tt_ % 7;
  u32x4 tx[2], gwv;
  {
    const rsrc_t rsX = mkrs(p.x, S * D * 2);
#pragma unroll
    for (int i = 0; i < 2; ++i) ald16(tx[i], rsX, (u32)((tid >> 3) * D * 2 + 16 * (tid & 7)), (u32)((64 * trt + 32 * i) * D * 2 + 128 * tcg));
    ald16(gwv, mkrs(p.gatew, D * 2), (u32)(16 * (tid & 7)), (u32)(128 * tcg));
  }
#define FLAGPOS 1
#define PINKO 0
#define BEPI 1
#define GDOT 0
#define CSLEEP 0
#define YRST_POS 3
#define T_POS 1
  const u32 yb0 = WS_YB + (u32)pair * YBS + (u32)tid * 16;
  auto yreset = [&]() { st16_sc1(rsws, yb0, (u32x4){SENT, SENT, SENT, SENT}); st16_sc1(rsws, yb0 + 4096, (u32x4){SENT, SENT, SENT, SENT}); asm volatile("" ::: "memory"); };
  if (YRST_POS == 0) yreset();
  u32 ko[11];
#pragma unroll
  for (int e = 0; e < 11; ++e) { ko[e] = LB_KC + kpos(2047 - 8 * tid - e); if (PINKO) asm volatile("" : "+v"(ko[e])); }
#define WOPF 0
  u32x4 pf[4];
  auto wopf = [&]() {
    const rsrc_t rs = mkrs(p.wo, D * D * 2);
#pragma unroll
    for (int i = 0; i < 4; ++i) ald16(pf[i], rs, (u32)(tid + 256 * i) * 16 + ((tid + 256 * i) < 896 ? 0u : OOB), (u32)o * 14336);
  };
  u32 tag;
  auto tphase = [&]() {
    asm volatile("s_waitcnt vmcnt(%3)" : "+v"(tx[0]), "+v"(tx[1]), "+v"(gwv) : "n"(YRST_POS == 0 ? 2 : 0) : "memory");
    LAS char* tt = lds + LB_TT;
#pragma unroll
    for (int i = 0; i < 2; ++i) { const int row = 32 * i + (tid >> 3); *(LAS u32x4*)(tt + row * 128 + (((tid & 7) ^ (row & 7)) << 4)) = tx[i]; }
    u32 e2 = ev;
    asm volatile("" : "+v"(e2));
    tag = (u32)__builtin_amdgcn_readfirstlane(e2) + 1u;
    bar();
    const int q = l16 >> 2, pp = lane & 3;
#pragma unroll
    for (int i = 0; i < 2; ++i) {
      const int lc = g + 4 * i;
      const int ra = 8 * lc + q, rb = ra + 4;
      const int chunk = 2 * w + (pp >> 1), sub = (pp & 1) * 8;
      const u32x2 lo = tr_read(tt + ra * 128 + ((chunk ^ (ra & 7)) << 4) + sub);
      const u32x2 hi = tr_read(tt + rb * 128 + ((chunk ^ (rb & 7)) << 4) + sub);
      const u32 a0 = WS_XT + (u32)trt * XTS + (u32)(64 * tcg + 16 * w + l16) * 256 + (u32)(8 * i + g) * 16;
      st16_sc1(rsws, a0, (u32x4){lo.x, tag, lo.y, tag});
      st16_sc1(rsws, a0 + 64, (u32x4){hi.x, tag, hi.y, tag});
    }
#pragma unroll
    for (int i = 0; i < 2; ++i) {
      float s = 0.f;
#pragma unroll
      for (int k = 0; k < 4; ++k) {
        const u32 xa = tx[i][k], ga = gwv[k];
        if (GDOT) s = __builtin_amdgcn_fdot2_f32_bf16(__builtin_bit_cast(bf16x2, xa), __builtin_bit_cast(bf16x2, ga), s, false);
        else {
          const u32 r = pk_bf16(bf2f(xa) * bf2f(ga), bfhi(xa) * bfhi(ga));
          s += bf2f(r); s += bfhi(r);
        }
      }
      s += dpp<0xB1>(s); s += dpp<0x4E>(s); s += dpp<0x141>(s);
      st8_sc1(rsws, (tid & 7) == 0 ? WS_G + (u32)((trt * 7 + tcg) * 64 + 32 * i + (tid >> 3)) * 8 : OOB, (u32x2){f2u(s), tag});
    }
    asm volatile("" ::: "memory");
  };
  if (T_POS == 0) tphase();
  if (WOPF == 1) wopf();
  if (YRST_POS == 1) yreset();
#define BHI 2
#define BMID 1
  if (BHI == 0) SETPRIO(BMID);
  if (BKW != BK0) SETPRIO(BKW);
  if (BKW != BK) {
    u32 a_ = are, b_ = aim;
    asm volatile("" : "+v"(a_), "+v"(b_));
    SETPRIO(BK);
  }
  const float dt = rbf(__expf(ldt));
  const f32x2 al = (f32x2){bf2f(are), bfhi(are)} * (dt * LOG2E);
  const f32x2 th = (f32x2){bf2f(aim), bfhi(aim)} * dt * INV2PI;
  const f32x2 br = (f32x2){bf2f(bre), bfhi(bre)} * dt, bi = (f32x2){bf2f(bim), bfhi(bim)} * dt;
  const f32x2 cr = {bf2f(cre), bfhi(cre)}, ci = {bf2f(cim), bfhi(cim)};
  cpx2 W; W.re = cr * br - ci * bi; W.im = cr * bi + ci * br;
  const float wm0 = __builtin_amdgcn_sqrtf(W.re[0] * W.re[0] + W.im[0] * W.im[0]) * exp2f_(fmaxf(0.f, al[0] * 2048.f));
  const float wm1 = __builtin_amdgcn_sqrtf(W.re[1] * W.re[1] + W.im[1] * W.im[1]) * exp2f_(fmaxf(0.f, al[1] * 2048.f));
  const float wmax = wave_max(fmaxf(wm0, wm1));
  const float SR = pow2_scale(wmax, 14);
  W.re *= SR; W.im *= SR;
  LAS char* stgr = lds + LB_STG + w * 8704;
  LAS char* stg = stgr + lane * 8;
  LAS char* frd = stgr + l16 * 544 + 16 * g;
  {
    const cpx2 Z8 = cexp_m(al, -th, 8.f);
    const cpx2 Z16 = csqr(Z8), Z32 = csqr(Z16), Z64 = csqr(Z32);
    cpx2 C[8];
    C[0].re = (f32x2){1.f, 1.f}; C[0].im = (f32x2){0.f, 0.f};
    C[1] = Z8; C[2] = Z16; C[3] = cmul(Z8, Z16); C[4] = Z32; C[5] = cmul(Z32, Z8); C[6] = cmul(Z32, Z16); C[7] = cmul(Z32, C[3]);
#pragma unroll
    for (int j = 0; j < 8; ++j) put_row(stg, j, C[j]);
    put_row(stg, 8, Z64);
#pragma unroll
    for (int j = 1; j < 8; ++j) put_row(stg, 8 + j, cmul(Z64, C[j]));
  }
  u32x4 Bf[8], Af[8];
#pragma unroll
  for (int ks = 0; ks < 8; ++ks) Bf[ks] = *(LAS u32x4*)(frd + 64 * ks);
  __builtin_amdgcn_sched_barrier(0);
  if (T_POS == 1) tphase();
  if (WOPF == 2) wopf();
  if (YRST_POS == 2) yreset();
  const cpx2 Z1 = cexp_m(al, th, 1.f);
  const cpx2 Z2 = csqr(Z1), Z4 = csqr(Z2);
  cpx2 Sm[8];
  Sm[1] = Z1; Sm[2] = Z2; Sm[3] = cmul(Z1, Z2); Sm[4] = Z4; Sm[5] = cmul(Z4, Z1); Sm[6] = cmul(Z4, Z2); Sm[7] = cmul(Z4, Sm[3]);
  const cpx2 Z128 = cexp_m(al, th, 128.f);
  const cpx2 Z256 = csqr(Z128), Z512 = csqr(Z256);
  f32x4 kacc[3];
  auto kmma = [&]() {
    f32x4 a0 = {0, 0, 0, 0}, a1 = {0, 0, 0, 0};
#pragma unroll
    for (int ks = 0; ks < 8; ks += 2) { a0 = mfma_h(Af[ks], Bf[ks], a0); a1 = mfma_h(Af[ks + 1], Bf[ks + 1], a1); }
    return a0 + a1;
  };
  auto rdA = [&]() {
#pragma unroll
    for (int ks = 0; ks < 8; ++ks) Af[ks] = *(LAS u32x4*)(frd + 64 * ks);
    __builtin_amdgcn_sched_barrier(0);
  };
  {
    const cpx2 B1 = cmul(W, Z128);
    put_row(stg, 0, W);
#pragma unroll
    for (int i = 1; i < 8; ++i) put_row(stg, i, cmul(W, Sm[i]));
    put_row(stg, 8, B1);
#pragma unroll
    for (int i = 1; i < 8; ++i) put_row(stg, 8 + i, cmul(B1, Sm[i]));
  }
  rdA();
  const cpx2 Z384 = cmul(Z256, Z128);
  {
    const cpx2 B2 = cmul(W, Z256), B3 = cmul(W, Z384), B4 = cmul(W, Z512);
    const cpx2 B5 = cmul(B4, Z128), B6 = cmul(B4, Z256), B7 = cmul(B4, Z384);
    put_row(stg, 0, B2); put_row(stg, 1, cmul(B2, Sm[2])); put_row(stg, 2, cmul(B2, Sm[4])); put_row(stg, 3, cmul(B2, Sm[6]));
    put_row(stg, 4, B3); put_row(stg, 5, cmul(B3, Sm[2])); put_row(stg, 6, cmul(B3, Sm[4])); put_row(stg, 7, cmul(B3, Sm[6]));
    put_row(stg, 8, B4); put_row(stg, 9, cmul(B4, Sm[4])); put_row(stg, 10, B5); put_row(stg, 11, cmul(B5, Sm[4]));
    put_row(stg, 12, B6); put_row(stg, 13, cmul(B6, Sm[4])); put_row(stg, 14, B7); put_row(stg, 15, cmul(B7, Sm[4]));
    kacc[0] = kmma();
#pragma unroll
    for (int i = 0; i < 8; ++i) { __builtin_amdgcn_sched_group_barrier(0x8, 1, 0); __builtin_amdgcn_sched_group_barrier(0x202, 14, 0); }
  }
  rdA();
  if (BHI == 1) SETPRIO(BMID);
  if (T_POS == 2) tphase();
  if (WOPF == 3) wopf();
  if (YRST_POS == 3) yreset();
  {
    const cpx2 Z1024 = cexp_m(al, th, 1024.f);
    const cpx2 Z2048 = csqr(Z1024);
    const cpx2 B8 = cmul(W, Z1024);
    const cpx2 Z640 = cmul(Z512, Z128), Z768 = cmul(Z512, Z256), Z896 = cmul(Z512, Z384);
    put_row(stg, 0, B8); put_row(stg, 1, cmul(B8, Z128)); put_row(stg, 2, cmul(B8, Z256)); put_row(stg, 3, cmul(B8, Z384));
    put_row(stg, 4, cmul(B8, Z512)); put_row(stg, 5, cmul(B8, Z640)); put_row(stg, 6, cmul(B8, Z768)); put_row(stg, 7, cmul(B8, Z896));
    put_row(stg, 8, cmul(W, Z2048));
    kacc[1] = kmma();
#pragma unroll
    for (int i = 0; i < 8; ++i) { __builtin_amdgcn_sched_group_barrier(0x8, 1, 0); __builtin_amdgcn_sched_group_barrier(0x202, 12, 0); }
  }
  rdA();
  kacc[2] = kmma();
  u32x4 uv[4];
  const u32 uoff = WS_XT + (u32)(16 * half) * XTS + (u32)d * 256;
  const u32 uvo = (u32)(lane >> 4) * XTS + (u32)l16 * 16;
  {
    const float isr = rcpf_(SR);
    LAS char* kx = lds + LB_KT + w * 2048 + lane * 16;
    if (half == 0) { *(LAS f32x4*)(kx) = kacc[1] * isr; }
    else { *(LAS f32x4*)(kx) = kacc[0] * isr; *(LAS f32x4*)(kx + 1024) = kacc[2] * isr; }
    if (BHI == 2) SETPRIO(BMID);
#pragma unroll
    for (int i = 0; i < 4; ++i) ald16s(uv[i], rsws, uvo, uoff + i * 4 * XTS);
    if (FLAGPOS == 0) waitvm<4>();
    if (WOPF) { tie(pf[0]); tie(pf[1]); tie(pf[2]); tie(pf[3]); }
    bar();
  }
  if (FLAGPOS == 0 && tid == 0) st4_sc1(rsws, WS_SEEN + (u32)pair * 4, tag);
  {
    const float isr = rcpf_(SR);
    LAS char* kx = lds + LB_KT + (w ^ 1) * 2048 + lane * 16;
    LAS char* kc = lds + LB_KC + ch * 1536;
    if (half == 0) {
      const f32x4 t0 = kacc[0] * isr + *(LAS f32x4*)(kx);
      const f32x4 t2 = kacc[2] * isr + *(LAS f32x4*)(kx + 1024);
      *(LAS u32x2*)(kc + (4 * (g & 1) + 8 * l16 + 128 * (g >> 1)) * 2) = (u32x2){pk_bf16(t0[0], t0[1]), pk_bf16(t0[2], t0[3])};
      const u32 h01 = pk_bf16(t2[0], t2[1]), h23 = pk_bf16(t2[2], t2[3]);
      if (g < 2) {
        LAS char* q2 = kc + (512 + l16 + 64 * g) * 2;
        *(LAS u16*)(q2) = (u16)h01; *(LAS u16*)(q2 + 32) = (u16)(h01 >> 16); *(LAS u16*)(q2 + 64) = (u16)h23; *(LAS u16*)(q2 + 96) = (u16)(h23 >> 16);
      } else if (lane == 32) { *(LAS u32*)(kc + 640 * 2) = h01 & 0xffffu; }
    } else {
      const f32x4 t1 = kacc[1] * isr + *(LAS f32x4*)(kx);
      const u32 h01 = pk_bf16(t1[0], t1[1]), h23 = pk_bf16(t1[2], t1[3]);
      if (g < 2) *(LAS u32x2*)(kc + (256 + 4 * l16 + 64 * g) * 2) = (u32x2){h01, h23};
      else { LAS char* q1 = kc + (384 + 2 * l16 + 64 * (g - 2)) * 2; *(LAS u32*)(q1) = h01; *(LAS u32*)(q1 + 64) = h23; }
    }
  }
  bar();
  if (BHI == 4) SETPRIO(BMID);
  {
    u32 dw[2][6];
#pragma unroll
    for (int c = 0; c < 2; ++c) {
#pragma unroll
      for (int k = 0; k < 5; ++k) {
        u16x2 v;
        v.x = *(LAS u16*)(lds + c * 1536 + ko[2 * k]);
        v.y = *(LAS u16*)(lds + c * 1536 + ko[2 * k + 1]);
        dw[c][k] = __builtin_bit_cast(u32, v);
      }
      dw[c][5] = *(LAS u16*)(lds + c * 1536 + ko[10]);
    }
#pragma unroll
    for (int c = 0; c < 2; ++c) {
      u32 od[5];
#pragma unroll
      for (int k = 0; k < 5; ++k) od[k] = __builtin_amdgcn_alignbit(dw[c][k + 1], dw[c][k], 16);
      LAS char* cp = lds + LB_CP + c * CPCH + tid * 16;
      *(LAS u32x4*)(cp) = (u32x4){dw[c][0], dw[c][1], dw[c][2], dw[c][3]};
      *(LAS u32x4*)(cp + CPS) = (u32x4){od[0], od[1], od[2], od[3]};
      *(LAS u32x4*)(cp + 2 * CPS) = (u32x4){dw[c][1], dw[c][2], dw[c][3], dw[c][4]};
      *(LAS u32x4*)(cp + 3 * CPS) = (u32x4){od[1], od[2], od[3], od[4]};
    }
  }
  if (tid < 32) *(LAS u32x4*)(lds + LB_CP + (tid >> 2) * CPS + 4096 + (tid & 3) * 16) = (u32x4){0, 0, 0, 0};
  if (BHI == 5) SETPRIO(BMID);
  {
    waitv4<0>(uv[0], uv[1], uv[2], uv[3]);
    auto uv_ok = [&]() { bool ok = true;
#pragma unroll
      for (int i = 0; i < 4; ++i) ok &= (uv[i][1] == tag) & (uv[i][3] == tag);
      return __all(ok); };
    if (!uv_ok()) {
      u32 spins = 0;
#pragma unroll 1
      do {
        if (spin_fail(spins, rsws)) break;
#pragma unroll
        for (int i = 0; i < 4; ++i) uv[i] = ld16_sc1(rsws, uoff + i * 4 * XTS + uvo);
      } while (!uv_ok());
    }
    LAS char* ub = lds + LB_U + ch * 4608;
    const int sg = lane & 15;
    const int pi4 = 8 * (sg >> 3) + 2 * (sg & 3) + ((sg >> 2) & 1);
    LAS char* uw = ub + 512 + half * 2048 + (64 * (lane >> 4) + 4 * pi4) * 2;
#pragma unroll
    for (int i = 0; i < 4; ++i) { const u32 a = uv[i][0], b = uv[i][2]; *(LAS u32x2*)(uw + i * 512) = (u32x2){a, b}; }
    if (half == 0 && lane < 32) *(LAS u32x4*)(ub + lane * 16) = (u32x4){0, 0, 0, 0};
  }
  bar();
  if (FLAGPOS == 1 && tid == 0) st4_sc1(rsws, WS_SEEN + (u32)pair * 4, tag);
  if (BHI == 6) SETPRIO(BMID);
  if (CSLEEP) __builtin_amdgcn_s_sleep(CSLEEP);
  f32x4 acc[8];
#pragma unroll
  for (int i = 0; i < 8; ++i) acc[i] = (f32x4){0, 0, 0, 0};
  float sk[2][2][4];
  auto skip = [&]() {
    const float sd0 = bf2f(dsk2), sd1 = bfhi(dsk2);
#pragma unroll
    for (int ci = 0; ci < 2; ++ci) {
      const int l0 = 256 * (2 * w + ci) + 16 * l16 + 4 * g;
      const u32x2 u0 = *(LAS u32x2*)(lds + LB_U + 512 + l0 * 2);
      const u32x2 u1 = *(LAS u32x2*)(lds + LB_U + 4608 + 512 + l0 * 2);
      const u32 a0 = u0[0], a1 = u0[1], b0 = u1[0], b1 = u1[1];
      sk[ci][0][0] = rbf(bf2f(a0) * sd0); sk[ci][0][1] = rbf(bfhi(a0) * sd0); sk[ci][0][2] = rbf(bf2f(a1) * sd0); sk[ci][0][3] = rbf(bfhi(a1) * sd0);
      sk[ci][1][0] = rbf(bf2f(b0) * sd1); sk[ci][1][1] = rbf(bfhi(b0) * sd1); sk[ci][1][2] = rbf(bf2f(b1) * sd1); sk[ci][1][3] = rbf(bfhi(b1) * sd1);
    }
  };
  {
    const int cs = (3 - l16) & 3;
    LAS char* abase = lds + LB_CP + ch * CPCH + cs * CPS + (2031 - l16 + 8 * g - cs) * 2 - 512 * 7 - 64 * (4 * half + 3);
    LAS char* bbase = lds + LB_U + ch * 4608 + (256 + 16 * (l16 - 1) + 8 * g) * 2 - 64 * (4 * half + 3);
    LAS char* abase2 = abase + 8;
    asm volatile("" : "+v"(abase2));
    u32x4 A[2][8], B[2][8];
    auto ldfr = [&](int it, int buf) {
#pragma unroll
      for (int s = 0; s < 8; ++s) {
        const int off = 512 * (7 - s) + 64 * (3 - it);
        A[buf][s] = cat(*(LAS u32x2*)(abase + off), *(LAS u32x2*)(abase2 + off + CONVADJ));
        B[buf][s] = *(LAS u32x4*)(bbase + 512 * s + 64 * (3 - it));
      }
    };
    ldfr(0, 0);
    __builtin_amdgcn_sched_barrier(0);
#pragma unroll
    for (int it = 0; it < 4; ++it) {
      if (it < 3) ldfr(it + 1, (it + 1) & 1);
      if (BEPI && it == 3) skip();
#pragma unroll
      for (int s = 0; s < 8; ++s)
#pragma unroll
        for (int cb = 7; cb >= s; --cb) acc[cb] = mfma16(A[it & 1][s], B[it & 1][cb - s], acc[cb]);
      if (it < 3) {
#pragma unroll
        for (int i = 0; i < 24; ++i) { __builtin_amdgcn_sched_group_barrier(0x8, 1, 0); __builtin_amdgcn_sched_group_barrier(0x100, 1, 0); }
      } else if (BEPI) {
        __builtin_amdgcn_sched_group_barrier(0x100, 4, 0);
#pragma unroll
        for (int i = 0; i < 30; ++i) { __builtin_amdgcn_sched_group_barrier(0x8, 1, 0); __builtin_amdgcn_sched_group_barrier(0x2, 2, 0); }
#pragma unroll
        for (int ci = 0; ci < 2; ++ci)
#pragma unroll
          for (int c2 = 0; c2 < 2; ++c2) asm volatile("" : "+v"(sk[ci][c2][0]), "+v"(sk[ci][c2][1]), "+v"(sk[ci][c2][2]), "+v"(sk[ci][c2][3]));
      }
      __builtin_amdgcn_sched_barrier(0);
    }
  }
  SETPRIO(3);
  {
    f32x4 v0[4], v1[4];
    if (BEPI) {
      LAS char* exw = lds + LB_KC + w * 2048 + lane * 16;
#pragma unroll
      for (int pw = 0; pw < 4; ++pw) { *(LAS f32x4*)(exw + pw * 8192) = acc[2 * pw]; *(LAS f32x4*)(exw + pw * 8192 + 1024) = acc[2 * pw + 1]; }
      bar();
      LAS char* exr = lds + LB_KC + w * 8192 + lane * 16;
#pragma unroll
      for (int sw = 0; sw < 4; ++sw) { v0[sw] = *(LAS f32x4*)(exr + sw * 2048); v1[sw] = *(LAS f32x4*)(exr + sw * 2048 + 1024); }
    } else {
    LAS char* ex = lds + LB_EX;
    f32x4 own0 = acc[0], own1 = acc[1];
#pragma unroll
    for (int pw = 0; pw < 4; ++pw) {
      if (pw != w) {
        LAS char* dst = ex + ((pw * 3 + (w > pw ? w - 1 : w)) * 2) * 1024 + lane * 16;
        *(LAS f32x4*)(dst) = acc[2 * pw]; *(LAS f32x4*)(dst + 1024) = acc[2 * pw + 1];
      } else { own0 = acc[2 * pw]; own1 = acc[2 * pw + 1]; }
    }
    skip();
    bar();
#pragma unroll
    for (int sw = 0; sw < 4; ++sw) {
      if (sw != w) {
        LAS char* src = ex + ((w * 3 + (sw > w ? sw - 1 : sw)) * 2) * 1024 + lane * 16;
        v0[sw] = *(LAS f32x4*)(src); v1[sw] = *(LAS f32x4*)(src + 1024);
      } else { v0[sw] = own0; v1[sw] = own1; }
    }
    }
#pragma unroll
    for (int ci = 0; ci < 2; ++ci) {
      const int cb = 2 * w + ci;
      const f32x4 y0 = ci ? v1[0] + v1[1] : v0[0] + v0[1];
      const f32x4 y1 = ci ? v1[2] + v1[3] : v0[2] + v0[3];
      u32 P[4];
#pragma unroll
      for (int r = 0; r < 4; ++r) P[r] = pk_bf16(rbf(y0[r]) + sk[ci][0][r], rbf(y1[r]) + sk[ci][1][r]);
      st16_sc1(rsws, WS_YB + (u32)pair * YBS + (u32)(256 * cb + 16 * l16 + 4 * g) * 4, (u32x4){sane(P[0]), sane(P[1]), sane(P[2]), sane(P[3])});
    }
  }
}

DI void roleJ(rsrc_t rsws) {
  const int tid = threadIdx.x;
  if (tid >= 64) return;
  u32 ev = ld4_sc1(rsws, WS_EPOCH);
  asm volatile("" : "+v"(ev));
  const u32 tag = (u32)__builtin_amdgcn_readfirstlane(ev) + 1u;
  u32 spins = 0;
#pragma unroll 1
  for (;;) {
    __builtin_amdgcn_s_sleep(32);
    const u32x4 a = ld16_sc1(rsws, tid < 56 ? WS_SEEN + (u32)tid * 16 : WS_SEEN + 1024), b = ld16_sc1(rsws, WS_SEEN + 1024 + (u32)tid * 16);
    const bool ok = (a[0] == tag) & (a[1] == tag) & (a[2] == tag) & (a[3] == tag) & (b[0] == tag) & (b[1] == tag) & (b[2] == tag) & (b[3] == tag);
    if (__all(ok) || spin_fail(spins, rsws)) break;
  }
  if (tid == 0) st4_sc1(rsws, WS_EPOCH, tag);
}

#define IPF0 488
#define IPFS 64
__global__ void __launch_bounds__(256, 2) k_main(Params p) {
  extern __shared__ __attribute__((aligned(16))) char smem[];
  LAS char* lds = (LAS char*)smem;
  {
    u64 pc; u32 len, vo, vd;
    asm volatile("s_cmp_lt_u32 %4, %5\n\t"
                 "s_cbranch_scc1 .Lipf_skip\n\t"
                 "s_getpc_b64 %0\n"
                 ".Lipf_pc:\n\t"
                 "s_sub_u32 %1, %4, %5\n\t"
                 "s_lshr_b32 %1, %1, 3\n\t"
                 "s_mul_i32 %1, %1, %7\n\t"
                 "v_mul_u32_u24 %2, %8, %6\n\t"
                 "v_add_u32 %2, %1, %2\n\t"
                 "s_mov_b32 %1, .Lipf_end-.Lipf_pc\n\t"
                 "v_min_u32 %2, %1, %2\n\t"
                 "global_load_dword %3, %2, %0\n\t"
                 "s_waitcnt vmcnt(0) lgkmcnt(0)\n\t"
                 "s_endpgm\n"
                 ".Lipf_skip:"
                 : "=&s"(pc), "=&s"(len), "=&v"(vo), "=&v"(vd)
                 : "s"(blockIdx.x), "n"(IPF0), "v"(threadIdx.x), "n"(256 * IPFS), "n"(IPFS) : "scc");
    __builtin_amdgcn_sched_barrier(0);
  }
  asm volatile("" :: "s"(p.x), "s"(p.wqkv), "s"(p.wphi), "s"(p.wo), "s"(p.Are), "s"(p.Aim), "s"(p.Bre), "s"(p.Bim), "s"(p.Cre), "s"(p.Cim), "s"(p.logdt), "s"(p.dskip), "s"(p.gatew), "s"(p.out), "s"(p.ws));
  const rsrc_t rsws = mkrs(p.ws, WS_USED);
  const int b = blockIdx.x, xcd = b & 7, j = b >> 3;
  const int o = j & 31;
  if (j < 32) { roleA(p, xcd, o, lds, rsws); }
  else if (o < 28) { roleB(p, xcd, o, lds, rsws); }
  else if (b == 256 + 28 * 8) { roleJ(rsws); }
}

extern "C" void kernel_launch(void* const* d_in, const int* in_sizes, int n_in, void* d_out, int out_size, void* d_ws, size_t ws_size, hipStream_t stream) {
  Params p;
  p.x = (const u16*)d_in[0]; p.wqkv = (const u16*)d_in[1]; p.wphi = (const u16*)d_in[2]; p.wo = (const u16*)d_in[3];
  p.Are = (const u16*)d_in[5]; p.Aim = (const u16*)d_in[6]; p.Bre = (const u16*)d_in[7]; p.Bim = (const u16*)d_in[8];
  p.Cre = (const u16*)d_in[9]; p.Cim = (const u16*)d_in[10]; p.logdt = (const u16*)d_in[11]; p.dskip = (const u16*)d_in[12]; p.gatew = (const u16*)d_in[13];
  p.out = (u16*)d_out; p.ws = (char*)d_ws;
  (void)hipFuncSetAttribute((const void*)k_main, hipFuncAttributeMaxDynamicSharedMemorySize, LDS_BYTES);
  k_main<<<512, 256, LDS_BYTES, stream>>>(p);
}
```
